```python
import torch

def _softmax(x, axis=-1):
    return torch.softmax(x, dim=axis)
def _gelu(x):
    return torch.nn.functional.gelu(x, approximate="tanh")
def _silu(x):
    return torch.nn.functional.silu(x)
def _sigmoid(x):
    return torch.sigmoid(x)
def _layernorm(x, axis=-1, eps=1e-5):
    m = x.mean(dim=axis, keepdim=True)
    v = x.var(dim=axis, keepdim=True, unbiased=False)
    return (x - m) / torch.sqrt(v + eps)
def _scatter_add(tgt, idx, src, axis=0):
    return tgt.clone().index_add_(axis, idx, src)
def _segment_sum(src, seg, n):
    out = torch.zeros((n,) + src.shape[1:], dtype=src.dtype)
    return out.index_add_(0, seg, src)
def _pad(x, pw):
    flat = tuple(v for pair in reversed(pw) for v in pair)
    return torch.nn.functional.pad(x, flat)

kernel_name = 'parallel_sum__sliding_block_cached__dense_activation_silu__decode'

def setup_inputs():

    _w = (lambda a: (lambda t: t.to(torch.bfloat16) if t.is_floating_point() else t)(torch.as_tensor(a)))
    def _A_setup():
        B, Sq, D = 1, 1, 640
        Hq, Hkv, Sk, Dh = 32, 32, 4096, 128
        assert Hq % Hkv == 0, (Hq, Hkv)
        return {
            "x": _w((lambda *s: torch.randn(*s, dtype=torch.bfloat16) * 0.05)(B, Sq, D)),
            "k_cache": _w((lambda *s: torch.randn(*s, dtype=torch.bfloat16) * 3.2)(B, Hkv, Sk, Dh)),
            "v_cache": _w((lambda *s: torch.randn(*s, dtype=torch.bfloat16) * 0.8)(B, Hkv, Sk, Dh)),
            "wq": _w((lambda *s: torch.randn(*s, dtype=torch.bfloat16) * 3.2)(D, Hq * Dh) * (D ** -0.5)),
            "wo": _w((lambda *s: torch.randn(*s, dtype=torch.bfloat16) * 0.8)(Hq * Dh, D) * ((Hq * Dh) ** -0.5)),
        }
    _A_in = _A_setup()

    def _B_setup():
        B, S, D, F = 1, 1, 640, 1728
        return {
            "x": _w((lambda *s: torch.randn(*s, dtype=torch.bfloat16) * 0.05)(B, S, D)),
            "w1": _w((lambda *s: torch.randn(*s, dtype=torch.bfloat16) * 3.2)(D, F) * (D ** -0.5)),
            "w2": _w((lambda *s: torch.randn(*s, dtype=torch.bfloat16) * 0.4)(F, D) * (F ** -0.5)),
        }
    _B_in = _B_setup()

    return {
        "A_x": _A_in["x"],
        "A_k_cache": _A_in["k_cache"],
        "A_v_cache": _A_in["v_cache"],
        "A_wq": _A_in["wq"],
        "A_wo": _A_in["wo"],
        "B_x": _B_in["x"],
        "B_w1": _B_in["w1"],
        "B_w2": _B_in["w2"],
    }

def reference(A_x, A_k_cache, A_v_cache, A_wq, A_wo, B_x, B_w1, B_w2):
    def _A_ref(x, k_cache, v_cache, wq, wo):
        B, Sq, D = x.shape
        Hq, Dh = 32, 128
        Hkv, Sk = k_cache.shape[1], k_cache.shape[2]
        W = min(4096, max(256, Sk // 4))
        rep = Hq // Hkv
        q = (lambda x, *d: x.permute(*d))((x @ wq).reshape(B, Sq, Hq, Dh), 0, 2, 1, 3)
        k = (lambda x, n, axis: torch.repeat_interleave(x, n, dim=axis))(k_cache[:, :, Sk - W:, :], rep, axis=1)
        v = (lambda x, n, axis: torch.repeat_interleave(x, n, dim=axis))(v_cache[:, :, Sk - W:, :], rep, axis=1)
        scores = torch.einsum("bhqd,bhkd->bhqk", q, k) * (Dh ** -0.5)
        p = _softmax(scores, axis=-1)
        o = torch.einsum("bhqk,bhkd->bhqd", p, v)
        return (lambda x, *d: x.permute(*d))(o, 0, 2, 1, 3).reshape(B, Sq, Hq * Dh) @ wo

    def _B_ref(x, w1, w2):
        h = torch.einsum("bsd,df->bsf", x, w1)
        h = _silu(h)
        out = torch.einsum("bsf,fd->bsd", h, w2)
        return out

    x = A_x
    xn = _layernorm(x, axis=-1)
    return x + _A_ref(xn, A_k_cache, A_v_cache, A_wq, A_wo) + _B_ref(xn, B_w1, B_w2)
```

```cpp
#include <hip/hip_runtime.h>
#include <hip/hip_bf16.h>
typedef unsigned short u16;
__device__ __forceinline__ float bf2f(u16 v){ return __uint_as_float(((unsigned)v)<<16); }
__device__ __forceinline__ u16 f2bf(float f){ unsigned u=__float_as_uint(f); return (u16)((u+0x7fffu+((u>>16)&1u))>>16); }
__device__ __forceinline__ float rbf(float f){ return bf2f(f2bf(f)); }
constexpr int D=640, HQ=32, SK=4096, DH=128, W=1024, F=1728;

__device__ void layernorm(const u16* x, float* xn  , float* red){
  int t=threadIdx.x;
  float s=0; for(int i=t;i<D;i+=blockDim.x) s+=bf2f(x[i]);
  red[t]=s; __syncthreads();
  for(int o=blockDim.x/2;o>0;o>>=1){ if(t<o) red[t]+=red[t+o]; __syncthreads(); }
  float mean=red[0]/D; __syncthreads();
  float v=0; for(int i=t;i<D;i+=blockDim.x){ float d=bf2f(x[i])-mean; v+=d*d; }
  red[t]=v; __syncthreads();
  for(int o=blockDim.x/2;o>0;o>>=1){ if(t<o) red[t]+=red[t+o]; __syncthreads(); }
  float var=red[0]/D; __syncthreads();
  float m=rbf(mean), vv=rbf(var);
  float den=rbf(sqrtf(rbf(vv+1e-5f)));
  for(int i=t;i<D;i+=blockDim.x) xn[i]=rbf(rbf(bf2f(x[i])-m)/den);
  __syncthreads();
}
__global__ void __launch_bounds__(256) k1(const u16* ax,const u16* wq,const u16* w1,float* q,float* h){
  __shared__ float xn[D]; __shared__ float red[256];
  layernorm(ax,xn,red);
  int n=blockIdx.x*256+threadIdx.x;
  if(n<4096){ float a=0; for(int k=0;k<D;k++) a+=xn[k]*bf2f(wq[(size_t)k*4096+n]); q[n]=rbf(a);}
  else if(n-4096<F){ int f=n-4096; float a=0; for(int k=0;k<D;k++) a+=xn[k]*bf2f(w1[(size_t)k*F+f]); a=rbf(a); h[f]=rbf(a/(1.f+__expf(-a))); }
}
__global__ void __launch_bounds__(256) k2(const float* q,const u16* kc,const u16* vc,float* o){
  __shared__ float p[W]; __shared__ float red[256]; __shared__ float qs[DH];
  int h=blockIdx.x,t=threadIdx.x;
  if(t<DH) qs[t]=q[h*DH+t]; __syncthreads();
  const u16* K=kc+((size_t)h*SK+(SK-W))*DH; const u16* V=vc+((size_t)h*SK+(SK-W))*DH;
  float mx=-1e30f;
  for(int j=t;j<W;j+=256){ float a=0; for(int d=0;d<DH;d++) a+=qs[d]*bf2f(K[(size_t)j*DH+d]); a=rbf(rbf(a)*rbf(0.08838834764831845f)); p[j]=a; mx=fmaxf(mx,a);}
  red[t]=mx; __syncthreads();
  for(int s=128;s>0;s>>=1){ if(t<s) red[t]=fmaxf(red[t],red[t+s]); __syncthreads(); }
  mx=red[0]; __syncthreads();
  float l=0; for(int j=t;j<W;j+=256){ float e=__expf(p[j]-mx); p[j]=e; l+=e; }
  red[t]=l; __syncthreads();
  for(int s=128;s>0;s>>=1){ if(t<s) red[t]+=red[t+s]; __syncthreads(); }
  l=red[0];
  if(t<DH){ float a=0; for(int j=0;j<W;j++) a+=rbf(p[j]/l)*bf2f(V[(size_t)j*DH+t]); o[h*DH+t]=rbf(a);}
}
__global__ void __launch_bounds__(64) k3(const u16* ax,const float* o,const float* h,const u16* wo,const u16* w2,u16* out){
  int n=blockIdx.x*64+threadIdx.x;
  float a=0; for(int k=0;k<4096;k++) a+=o[k]*bf2f(wo[(size_t)k*D+n]);
  float b=0; for(int k=0;k<F;k++) b+=h[k]*bf2f(w2[(size_t)k*D+n]);
  out[n]=f2bf(rbf(bf2f(ax[n])+rbf(a))+rbf(b));
}
extern "C" void kernel_launch(void* const* d_in, const int* in_sizes, int n_in, void* d_out, int out_size, void* d_ws, size_t ws_size, hipStream_t stream){
  const u16* ax=(const u16*)d_in[0]; const u16* kc=(const u16*)d_in[1]; const u16* vc=(const u16*)d_in[2];
  const u16* wq=(const u16*)d_in[3]; const u16* wo=(const u16*)d_in[4]; const u16* w1=(const u16*)d_in[6]; const u16* w2=(const u16*)d_in[7];
  float* q=(float*)d_ws; float* h=q+4096; float* o=h+2048;
  k1<<<(4096+F+255)/256,256,0,stream>>>(ax,wq,w1,q,h);
  k2<<<HQ,256,0,stream>>>(q,kc,vc,o);
  k3<<<D/64,64,0,stream>>>(ax,o,h,wo,w2,(u16*)d_out);
}
```

```cpp
#include <hip/hip_runtime.h>
#include <hip/hip_bf16.h>

typedef unsigned short u16;
typedef unsigned int u32;
typedef unsigned long long u64;
typedef u32 v4u __attribute__((ext_vector_type(4)));
typedef u32 v2u __attribute__((ext_vector_type(2)));
typedef float f32x4 __attribute__((ext_vector_type(4)));
typedef __bf16 bf16x8 __attribute__((ext_vector_type(8)));
typedef __bf16 bf16x2 __attribute__((ext_vector_type(2)));
#define GAS __attribute__((address_space(1)))
#define LAS __attribute__((address_space(3)))

#define AUX_STREAM 2
#define AUX_SC1 16

constexpr int D_ = 640, NH = 32, SK = 4096, DH = 128, WIN = 1024, FF = 1728;
constexpr u32 WS_EPOCH = 0;
constexpr u32 WS_Q = 4096;
constexpr u32 WS_A = WS_Q + 32 * 8 * 48 * 16;
constexpr u32 WS_Y = WS_A + 32 * 8 * 48 * 16;
constexpr u32 WS_END = WS_Y + 256 * 160 * 16;
constexpr u32 WS_STAMP = 32u << 20;
constexpr u32 TAGM = 0x3FFu;
constexpr int SPIN_CAP = 200000;

struct Params {
  const u16* x; const u16* kc; const u16* vc; const u16* wq; const u16* wo; const u16* w1; const u16* w2;
  u16* out; unsigned char* ws;
};

#define SB() __builtin_amdgcn_sched_barrier(0)
#define BAR() do { SB(); asm volatile("s_waitcnt lgkmcnt(0)\n\ts_barrier" ::: "memory"); SB(); } while (0)
#define MEMFENCE() asm volatile("" ::: "memory")

#define ST(k)

__device__ __forceinline__ float asf(u32 x) { return __builtin_bit_cast(float, x); }
__device__ __forceinline__ u32 asu(float x) { return __builtin_bit_cast(u32, x); }
template <int CTRL> __device__ __forceinline__ float dpp(float x) {
  return asf(__builtin_amdgcn_mov_dpp(asu(x), CTRL, 0xf, 0xf, true));
}
__device__ __forceinline__ float row_sum16(float x) { x += dpp<0x128>(x); x += dpp<0x124>(x); x += dpp<0x122>(x); x += dpp<0x121>(x); return x; }
__device__ __forceinline__ float oct_sum(float x) { x += dpp<0xB1>(x); x += dpp<0x4E>(x); x += dpp<0x141>(x); return x; }
__device__ __forceinline__ float oct_max(float x) { x = fmaxf(x, dpp<0xB1>(x)); x = fmaxf(x, dpp<0x4E>(x)); x = fmaxf(x, dpp<0x141>(x)); return x; }
__device__ __forceinline__ float xrow_sum(float x) {
  auto s = __builtin_amdgcn_permlane16_swap(asu(x), asu(x), false, false);
  x = asf(s[0]) + asf(s[1]);
  auto t = __builtin_amdgcn_permlane32_swap(asu(x), asu(x), false, false);
  return asf(t[0]) + asf(t[1]);
}
__device__ __forceinline__ float xrow_max(float x) {
  auto s = __builtin_amdgcn_permlane16_swap(asu(x), asu(x), false, false);
  x = fmaxf(asf(s[0]), asf(s[1]));
  auto t = __builtin_amdgcn_permlane32_swap(asu(x), asu(x), false, false);
  return fmaxf(asf(t[0]), asf(t[1]));
}
__device__ __forceinline__ u32 pk_bf16(float lo, float hi) {
  bf16x2 r = {(__bf16)lo, (__bf16)hi};
  return __builtin_bit_cast(u32, r);
}
__device__ __forceinline__ float rbf(float x) { return asf(pk_bf16(x, x) << 16); }
__device__ __forceinline__ float lo_f(u32 w) { return asf(w << 16); }
__device__ __forceinline__ float hi_f(u32 w) { return asf(w & 0xffff0000u); }
__device__ __forceinline__ float dot2(u32 a, u32 b, float c) {
  return __builtin_amdgcn_fdot2_f32_bf16(__builtin_bit_cast(bf16x2, a), __builtin_bit_cast(bf16x2, b), c, false);
}
__device__ __forceinline__ f32x4 mfma(v4u a, v4u b, f32x4 c) {
  return __builtin_amdgcn_mfma_f32_16x16x32_bf16(__builtin_bit_cast(bf16x8, a), __builtin_bit_cast(bf16x8, b), c, 0, 0, 0);
}
struct Tag { u32 f0, f1, f2, f3; };
__device__ __forceinline__ v4u tag_pack(f32x4 v, const Tag& t) {
  v4u r;
  r.x = ((asu(v.x) + 0x200u) & ~TAGM) | t.f0; r.y = ((asu(v.y) + 0x200u) & ~TAGM) | t.f1;
  r.z = ((asu(v.z) + 0x200u) & ~TAGM) | t.f2; r.w = ((asu(v.w) + 0x200u) & ~TAGM) | t.f3;
  return r;
}
__device__ __forceinline__ u32 tag_bad(v4u g, const Tag& t) {
  return ((g.x ^ t.f0) | (g.y ^ t.f1) | (g.z ^ t.f2) | (g.w ^ t.f3)) & TAGM;
}
__device__ __forceinline__ bool any_lane(u32 bad) { return __builtin_amdgcn_ballot_w64(bad != 0) != 0; }
__device__ __forceinline__ f32x4 tag_strip(v4u g) {
  f32x4 r = {asf(g.x & ~TAGM), asf(g.y & ~TAGM), asf(g.z & ~TAGM), asf(g.w & ~TAGM)};
  return r;
}
__device__ __forceinline__ v4u amask(u32 dup, const v4u& m) { v4u r = {dup & m.x, dup & m.y, dup & m.z, dup & m.w}; return r; }
#define RSRC(ptr, bytes) __builtin_amdgcn_make_buffer_rsrc((void*)(ptr), 0, (int)(bytes), 0x00020000)
__device__ __forceinline__ v4u ldb(__amdgpu_buffer_rsrc_t r, u32 voff, u32 soff) { return __builtin_amdgcn_raw_buffer_load_b128(r, voff, soff, AUX_STREAM); }
__device__ __forceinline__ v4u ldc(__amdgpu_buffer_rsrc_t r, u32 voff, u32 soff) { return __builtin_amdgcn_raw_buffer_load_b128(r, voff, soff, AUX_SC1); }
__device__ __forceinline__ void stc(v4u v, __amdgpu_buffer_rsrc_t r, u32 voff, u32 soff) { __builtin_amdgcn_raw_buffer_store_b128(v, r, voff, soff, AUX_SC1); }

constexpr float LOG2E = 1.4426950408889634f;

__global__ void __launch_bounds__(512, 4) fused_decode(Params p) {
  __shared__ __attribute__((aligned(16))) float lds[5 * 128 + 10 * 64 + 64 + 64 + 8 * 128 + 16];
  LAS float* qpart = (LAS float*)lds;
  LAS float* hpart = qpart + 5 * 128;
  LAS u32* qb = (LAS u32*)(hpart + 10 * 64);
  LAS u32* hb = qb + 64;
  LAS float* ow = (LAS float*)(hb + 64);
  LAS float* ml = ow + 8 * 128;

  const int tid = threadIdx.x, lane = tid & 63;
  const int wave = __builtin_amdgcn_readfirstlane(tid >> 6);
  const int b = blockIdx.x, h = (b & 7) * 4 + (b >> 6), j = (b >> 3) & 7;
  const int c = lane & 15, g = lane >> 4;
  const bool heavy = wave < 5;
  const bool ffn = h < 27;
  ST(0);

  const __amdgpu_buffer_rsrc_t rx = RSRC(p.x, D_ * 2);
  const __amdgpu_buffer_rsrc_t rwq = RSRC(p.wq, heavy ? D_ * 4096 * 2 : 0);
  const __amdgpu_buffer_rsrc_t rw1 = RSRC(p.w1, (heavy && ffn) ? D_ * FF * 2 : 0);
  const __amdgpu_buffer_rsrc_t rk = RSRC(p.kc, NH * SK * DH * 2);
  const __amdgpu_buffer_rsrc_t rv = RSRC(p.vc, NH * SK * DH * 2);
  const __amdgpu_buffer_rsrc_t rwo = RSRC(p.wo, heavy ? 4096 * D_ * 2 : 0);
  const __amdgpu_buffer_rsrc_t rw2 = RSRC(p.w2, (heavy && ffn) ? FF * D_ * 2 : 0);
  const __amdgpu_buffer_rsrc_t rws = RSRC(p.ws, WS_END);

  const u64 epoch = __hip_atomic_load((GAS u64*)(p.ws + WS_EPOCH), __ATOMIC_RELAXED, __HIP_MEMORY_SCOPE_AGENT);
  const v4u xa = __builtin_amdgcn_raw_buffer_load_b128(rx, lane * 16, 0, 0);
  const v4u xb = __builtin_amdgcn_raw_buffer_load_b128(rx, 1024 + lane * 16, 0, 0);
  const int row0 = 80 * j + 16 * (heavy ? wave : 0);
  const v2u xo = __builtin_amdgcn_raw_buffer_load_b64(rx, g * 8, row0 * 2, 0);
  const v2u xr = __builtin_amdgcn_raw_buffer_load_b64(rx, 0, (b < 160 ? b : 0) * 8, 0);
  SB();
  v4u aq[4], ah[2];
#pragma unroll
  for (int s = 0; s < 4; ++s) aq[s] = ldb(rwq, (4 * g + s) * 8192 + c * 16, row0 * 8192 + h * 256);
#pragma unroll
  for (int s = 0; s < 2; ++s) ah[s] = ldb(rw1, (4 * g + 2 * (c >> 3) + s) * (FF * 2) + (c & 7) * 16, row0 * (FF * 2) + h * 128);
  SB();
  const u32 kvs = (u32)((h * SK + (SK - WIN) + 128 * j + 16 * wave) * 256);
  const u32 kvo = g * 256 + c * 16;
  v4u kk[4], vv[4];
#pragma unroll
  for (int i = 0; i < 4; ++i) kk[i] = ldb(rk, kvo + i * 1024, kvs);
  SB();
  ST(1);

  const u64 T = epoch + 1;
  Tag tg;
  {
    const u32 tl = __builtin_amdgcn_readfirstlane((u32)T), th = __builtin_amdgcn_readfirstlane((u32)(T >> 32));
    const u64 Tu = ((u64)th << 32) | tl;
    tg.f0 = tl & TAGM; tg.f1 = ((u32)(Tu >> 10) + tl) & TAGM; tg.f2 = ((u32)(Tu >> 20) + tl) & TAGM; tg.f3 = ((u32)(Tu >> 30) + tl) & TAGM;
  }
  v4u msk;
  {
    const u32 half = (c & 1) ? 0xffff0000u : 0x0000ffffu; const int r = (c & 7) >> 1;
    msk.x = r == 0 ? half : 0u; msk.y = r == 1 ? half : 0u; msk.z = r == 2 ? half : 0u; msk.w = r == 3 ? half : 0u;
  }
  u32 xd[4];
  {
    float xv[16];
#pragma unroll
    for (int k = 0; k < 4; ++k) { xv[2 * k] = lo_f(xa[k]); xv[2 * k + 1] = hi_f(xa[k]); xv[8 + 2 * k] = lo_f(xb[k]); xv[9 + 2 * k] = hi_f(xb[k]); }
    float s = 0.f;
#pragma unroll
    for (int k = 0; k < 16; ++k) s += xv[k];
    const float mean = xrow_sum(row_sum16(s)) * (1.f / D_);
    float v1 = 0.f, v2 = 0.f;
#pragma unroll
    for (int k = 0; k < 8; ++k) { const float d1 = xv[k] - mean, d2 = xv[8 + k] - mean; v1 = fmaf(d1, d1, v1); v2 = fmaf(d2, d2, v2); }
    const float var = xrow_sum(row_sum16(lane < 16 ? v1 + v2 : v1)) * (1.f / D_);
    const float mb = rbf(mean), den = rbf(sqrtf(rbf(rbf(var) + 1e-5f)));
    const float o0 = rbf(lo_f(xo.x) - mb) / den, o1 = rbf(hi_f(xo.x) - mb) / den, o2 = rbf(lo_f(xo.y) - mb) / den, o3 = rbf(hi_f(xo.y) - mb) / den;
    xd[0] = pk_bf16(o0, o0); xd[1] = pk_bf16(o1, o1); xd[2] = pk_bf16(o2, o2); xd[3] = pk_bf16(o3, o3);
  }
  ST(2);
  {
    f32x4 dq = {0.f, 0.f, 0.f, 0.f}, dh = {0.f, 0.f, 0.f, 0.f};
#pragma unroll
    for (int s = 0; s < 4; ++s) dq = mfma(amask(xd[s], msk), aq[s], dq);
    const u32 h0 = (c >> 3) ? xd[2] : xd[0], h1 = (c >> 3) ? xd[3] : xd[1];
    dh = mfma(amask(h0, msk), ah[0], dh);
    dh = mfma(amask(h1, msk), ah[1], dh);
    if (heavy) {
      if (lane < 32) *(LAS f32x4*)(qpart + wave * 128 + 8 * c + 4 * g) = dq;
      if ((c >> 3) == (g >> 1)) *(LAS f32x4*)(hpart + (2 * wave + (g >> 1)) * 64 + 8 * (c & 7) + 4 * (g & 1)) = dh;
    }
  }
  ST(3);
  BAR();
  if (wave == 7) {
    f32x4 a = {0.f, 0.f, 0.f, 0.f};
    if (lane < 32) {
#pragma unroll
      for (int w = 0; w < 5; ++w) a += *(LAS f32x4*)(qpart + w * 128 + 4 * lane);
    } else if (lane < 48) {
#pragma unroll
      for (int w = 0; w < 10; ++w) a += *(LAS f32x4*)(hpart + w * 64 + 4 * (lane - 32));
    }
    if (lane < 48) stc(tag_pack(a, tg), rws, lane * 16, WS_Q + (h * 8 + j) * 768);
  }
  BAR();
  ST(4);
#pragma unroll
  for (int i = 0; i < 4; ++i) vv[i] = ldb(rv, kvo + i * 1024, kvs);
  SB();
  asm volatile("" ::"v"(kk[0]), "v"(kk[1]), "v"(kk[2]), "v"(kk[3]));
  SB();
  ST(5);
  const int gi = 6 * wave + ((lane >> 3) < 6 ? (lane >> 3) : 5);
  const u32 qoff = (lane & 7) * 768 + gi * 16;
  v4u gq = ldc(rws, qoff, WS_Q + h * 8 * 768);
  SB();
  v4u bo[4], b2[2];
#pragma unroll
  for (int s = 0; s < 4; ++s) bo[s] = ldb(rwo, (4 * g + s) * 1280 + c * 16, (h * 128 + 16 * j) * 1280 + wave * 256);
#pragma unroll
  for (int s = 0; s < 2; ++s) b2[s] = ldb(rw2, (2 * g + s) * 1280 + c * 16, (h * 64 + 8 * j) * 1280 + wave * 256);
  SB();
  if (__builtin_expect(any_lane(tag_bad(gq, tg)), 0)) {
    int spin = 0;
    do { MEMFENCE(); gq = ldc(rws, qoff, WS_Q + h * 8 * 768); } while (any_lane(tag_bad(gq, tg)) && ++spin < SPIN_CAP);
  }
  {
    f32x4 v = tag_strip(gq);
    v.x = oct_sum(v.x); v.y = oct_sum(v.y); v.z = oct_sum(v.z); v.w = oct_sum(v.w);
    if ((lane & 7) == 0 && lane < 48) {
      if (gi < 32) { v2u q2 = {pk_bf16(v.x, v.y), pk_bf16(v.z, v.w)}; *(LAS v2u*)(qb + 2 * gi) = q2; }
      else {
        v4u hv;
#pragma unroll
        for (int k = 0; k < 4; ++k) { const float t = rbf(v[k]); const float sl = t / (1.f + __expf(-t)); hv[k] = pk_bf16(sl, sl); }
        *(LAS v4u*)(hb + 4 * (gi - 32)) = hv;
      }
    }
  }
  ST(6);
  BAR();
  {
    const v4u qf = *(LAS v4u*)(qb + 4 * c);
    float s[4];
#pragma unroll
    for (int i = 0; i < 4; ++i) {
      float d = dot2(kk[i].x, qf.x, 0.f); d = dot2(kk[i].y, qf.y, d); d = dot2(kk[i].z, qf.z, d); d = dot2(kk[i].w, qf.w, d);
      s[i] = rbf(rbf(row_sum16(d)) * 0.08838834764831845f);
    }
    const float m = xrow_max(fmaxf(fmaxf(s[0], s[1]), fmaxf(s[2], s[3])));
    float l = 0.f; f32x4 o = {0.f, 0.f, 0.f, 0.f};
#pragma unroll
    for (int i = 0; i < 4; ++i) { const float pe = __builtin_amdgcn_exp2f((s[i] - m) * LOG2E); l += pe; o = mfma(amask(pk_bf16(pe, pe), msk), vv[i], o); }
    l = xrow_sum(l);
    if (lane < 32) *(LAS f32x4*)(ow + wave * 128 + 8 * c + 4 * g) = o;
    if (lane == 0) { ml[2 * wave] = m; ml[2 * wave + 1] = l; }
  }
  ST(7);
  BAR();
  {
    const int ww = lane & 7;
    f32x4 o = *(LAS f32x4*)(ow + ww * 128 + 16 * wave + 4 * g);
    const float m = ml[2 * ww], l = ml[2 * ww + 1];
    const float M = oct_max(m), f = __builtin_amdgcn_exp2f((m - M) * LOG2E);
    o.x = oct_sum(o.x * f); o.y = oct_sum(o.y * f); o.z = oct_sum(o.z * f); o.w = oct_sum(o.w * f);
    const float L = oct_sum(l * f);
    const u32 base = WS_A + (h * 8 + j) * 768;
    if (c == 0) stc(tag_pack(o, tg), rws, (4 * wave + g) * 16, base);
    if (wave == 0 && lane == 1) { f32x4 t = {M, L, M, L}; stc(tag_pack(t, tg), rws, 32 * 16, base); }
  }
  ST(8);
  if (heavy) {
    const u32 aoff = (lane & 7) * 768 + (4 * j + g) * 16, moff = (lane & 7) * 768 + 32 * 16, abase = WS_A + h * 8 * 768;
    const v2u hv = *(LAS v2u*)(hb + 8 * j + 2 * g);
    v4u go, gm; int spin = 0;
    do { MEMFENCE(); go = ldc(rws, aoff, abase); gm = ldc(rws, moff, abase); } while (any_lane(tag_bad(go, tg) | tag_bad(gm, tg)) && ++spin < SPIN_CAP);
    ST(9);
    f32x4 o = tag_strip(go); const f32x4 mlv = tag_strip(gm);
    const float M = oct_max(mlv.x), f = __builtin_amdgcn_exp2f((mlv.x - M) * LOG2E);
    const float inv = 1.f / oct_sum(mlv.y * f);
    f32x4 y = {0.f, 0.f, 0.f, 0.f};
    y = mfma(amask(hv.x, msk), b2[0], y);
    y = mfma(amask(hv.y, msk), b2[1], y);
#pragma unroll
    for (int s = 0; s < 4; ++s) { const float t = oct_sum(o[s] * f) * inv; y = mfma(amask(pk_bf16(t, t), msk), bo[s], y); }
    if (lane < 32) stc(tag_pack(y, tg), rws, (32 * wave + 2 * c + g) * 16, WS_Y + b * 2560);
    ST(10);
  } else if (wave == 7 && b < 160) {
    v4u gy[4]; u32 bad;
    int spin = 0;
    do {
      MEMFENCE();
#pragma unroll
      for (int k = 0; k < 4; ++k) gy[k] = ldc(rws, (lane + 64 * k) * 2560, WS_Y + b * 16);
      bad = tag_bad(gy[0], tg) | tag_bad(gy[1], tg) | tag_bad(gy[2], tg) | tag_bad(gy[3], tg);
    } while (any_lane(bad) && ++spin < SPIN_CAP);
    ST(11);
    f32x4 a = (tag_strip(gy[0]) + tag_strip(gy[1])) + (tag_strip(gy[2]) + tag_strip(gy[3]));
    a.x = xrow_sum(row_sum16(a.x)); a.y = xrow_sum(row_sum16(a.y)); a.z = xrow_sum(row_sum16(a.z)); a.w = xrow_sum(row_sum16(a.w));
    if (lane == 0) {
      v2u o = {pk_bf16(a.x + lo_f(xr.x), a.y + hi_f(xr.x)), pk_bf16(a.z + lo_f(xr.y), a.w + hi_f(xr.y))};
      *(GAS v2u*)(p.out + 4 * b) = o;
      if (b == 0) __hip_atomic_store((GAS u64*)(p.ws + WS_EPOCH), T, __ATOMIC_RELAXED, __HIP_MEMORY_SCOPE_AGENT);
    }
    ST(12);
  }
}

extern "C" void kernel_launch(void* const* d_in, const int* in_sizes, int n_in, void* d_out, int out_size, void* d_ws, size_t ws_size, hipStream_t stream) {
  Params p;
  p.x = (const u16*)d_in[0]; p.kc = (const u16*)d_in[1]; p.vc = (const u16*)d_in[2]; p.wq = (const u16*)d_in[3]; p.wo = (const u16*)d_in[4];
  p.w1 = (const u16*)d_in[6]; p.w2 = (const u16*)d_in[7]; p.out = (u16*)d_out; p.ws = (unsigned char*)d_ws;
  fused_decode<<<256, 512, 0, stream>>>(p);
}
```

```cpp
#define STAMP
#define AMP_G 0
#define AMP_FROM 0
#define AMP_TO 0
#include <hip/hip_runtime.h>
#include <hip/hip_bf16.h>

typedef unsigned short u16;
typedef unsigned int u32;
typedef unsigned long long u64;
typedef u32 v4u __attribute__((ext_vector_type(4)));
typedef u32 v2u __attribute__((ext_vector_type(2)));
typedef float f32x4 __attribute__((ext_vector_type(4)));
typedef __bf16 bf16x8 __attribute__((ext_vector_type(8)));
typedef __bf16 bf16x2 __attribute__((ext_vector_type(2)));
#define GAS __attribute__((address_space(1)))
#define LAS __attribute__((address_space(3)))

#define AUX_STREAM 2
#define AUX_SC1 16

constexpr int D_ = 640, NH = 32, SK = 4096, DH = 128, WIN = 1024, FF = 1728;
constexpr u32 WS_EPOCH = 0;
constexpr u32 WS_Q = 4096;
constexpr u32 WS_A = WS_Q + 32 * 8 * 48 * 16;
constexpr u32 WS_Y = WS_A + 32 * 8 * 48 * 16;
constexpr u32 WS_END = WS_Y + 256 * 160 * 16;
constexpr u32 WS_STAMP = 32u << 20;
constexpr u32 TAGM = 0x3FFu;
constexpr int SPIN_CAP = 200000;

struct Params {
  const u16* x; const u16* kc; const u16* vc; const u16* wq; const u16* wo; const u16* w1; const u16* w2;
  u16* out; unsigned char* ws;
};

#define SB() __builtin_amdgcn_sched_barrier(0)
#define BAR() do { SB(); asm volatile("s_waitcnt lgkmcnt(0)\n\ts_barrier" ::: "memory"); SB(); } while (0)
#define MEMFENCE() asm volatile("" ::: "memory")

#define NSTAMP 13
#define ST(k) do { SB(); asm volatile("s_memrealtime %0\n\ts_waitcnt lgkmcnt(0)" : "=s"(st_[k])::"memory"); SB(); } while (0)
#define ST_DECL u64 st_[NSTAMP]; _Pragma("unroll") for (int k_ = 0; k_ < NSTAMP; ++k_) st_[k_] = 0
#define ST_FLUSH() do { if (lane == 0) { _Pragma("unroll") for (int k_ = 0; k_ < NSTAMP; ++k_) ((GAS u64*)(p.ws + WS_STAMP))[(blockIdx.x * 8 + wave) * NSTAMP + k_] = st_[k_]; } } while (0)

__device__ __forceinline__ float asf(u32 x) { return __builtin_bit_cast(float, x); }
__device__ __forceinline__ u32 asu(float x) { return __builtin_bit_cast(u32, x); }
template <int CTRL> __device__ __forceinline__ float dpp(float x) {
  return asf(__builtin_amdgcn_mov_dpp(asu(x), CTRL, 0xf, 0xf, true));
}
__device__ __forceinline__ float row_sum16(float x) { x += dpp<0x128>(x); x += dpp<0x124>(x); x += dpp<0x122>(x); x += dpp<0x121>(x); return x; }
__device__ __forceinline__ float oct_sum(float x) { x += dpp<0xB1>(x); x += dpp<0x4E>(x); x += dpp<0x141>(x); return x; }
__device__ __forceinline__ float oct_max(float x) { x = fmaxf(x, dpp<0xB1>(x)); x = fmaxf(x, dpp<0x4E>(x)); x = fmaxf(x, dpp<0x141>(x)); return x; }
__device__ __forceinline__ float xrow_sum(float x) {
  auto s = __builtin_amdgcn_permlane16_swap(asu(x), asu(x), false, false);
  x = asf(s[0]) + asf(s[1]);
  auto t = __builtin_amdgcn_permlane32_swap(asu(x), asu(x), false, false);
  return asf(t[0]) + asf(t[1]);
}
__device__ __forceinline__ float xrow_max(float x) {
  auto s = __builtin_amdgcn_permlane16_swap(asu(x), asu(x), false, false);
  x = fmaxf(asf(s[0]), asf(s[1]));
  auto t = __builtin_amdgcn_permlane32_swap(asu(x), asu(x), false, false);
  return fmaxf(asf(t[0]), asf(t[1]));
}
__device__ __forceinline__ u32 pk_bf16(float lo, float hi) {
  bf16x2 r = {(__bf16)lo, (__bf16)hi};
  return __builtin_bit_cast(u32, r);
}
__device__ __forceinline__ float rbf(float x) { return asf(pk_bf16(x, x) << 16); }
__device__ __forceinline__ float lo_f(u32 w) { return asf(w << 16); }
__device__ __forceinline__ float hi_f(u32 w) { return asf(w & 0xffff0000u); }
__device__ __forceinline__ float dot2(u32 a, u32 b, float c) {
  return __builtin_amdgcn_fdot2_f32_bf16(__builtin_bit_cast(bf16x2, a), __builtin_bit_cast(bf16x2, b), c, false);
}
__device__ __forceinline__ f32x4 mfma(v4u a, v4u b, f32x4 c) {
  return __builtin_amdgcn_mfma_f32_16x16x32_bf16(__builtin_bit_cast(bf16x8, a), __builtin_bit_cast(bf16x8, b), c, 0, 0, 0);
}
struct Tag { u32 f0, f1, f2, f3; };
__device__ __forceinline__ v4u tag_pack(f32x4 v, const Tag& t) {
  v4u r;
  r.x = ((asu(v.x) + 0x200u) & ~TAGM) | t.f0; r.y = ((asu(v.y) + 0x200u) & ~TAGM) | t.f1;
  r.z = ((asu(v.z) + 0x200u) & ~TAGM) | t.f2; r.w = ((asu(v.w) + 0x200u) & ~TAGM) | t.f3;
  return r;
}
__device__ __forceinline__ u32 tag_bad(v4u g, const Tag& t) {
  return ((g.x ^ t.f0) | (g.y ^ t.f1) | (g.z ^ t.f2) | (g.w ^ t.f3)) & TAGM;
}
__device__ __forceinline__ bool any_lane(u32 bad) { return __builtin_amdgcn_ballot_w64(bad != 0) != 0; }
__device__ __forceinline__ f32x4 tag_strip(v4u g) {
  f32x4 r = {asf(g.x & ~TAGM), asf(g.y & ~TAGM), asf(g.z & ~TAGM), asf(g.w & ~TAGM)};
  return r;
}
__device__ __forceinline__ v4u amask(u32 dup, const v4u& m) { v4u r = {dup & m.x, dup & m.y, dup & m.z, dup & m.w}; return r; }
#define RSRC(ptr, bytes) __builtin_amdgcn_make_buffer_rsrc((void*)(ptr), 0, (int)(bytes), 0x00020000)
__device__ __forceinline__ v4u ldb(__amdgpu_buffer_rsrc_t r, u32 voff, u32 soff) { return __builtin_amdgcn_raw_buffer_load_b128(r, voff, soff, AUX_STREAM); }
__device__ __forceinline__ v4u ldc(__amdgpu_buffer_rsrc_t r, u32 voff, u32 soff) { return __builtin_amdgcn_raw_buffer_load_b128(r, voff, soff, AUX_SC1); }
__device__ __forceinline__ void stc(v4u v, __amdgpu_buffer_rsrc_t r, u32 voff, u32 soff) { __builtin_amdgcn_raw_buffer_store_b128(v, r, voff, soff, AUX_SC1); }

constexpr float LOG2E = 1.4426950408889634f;

__global__ void __launch_bounds__(512, 4) fused_decode(Params p) {
  __shared__ __attribute__((aligned(16))) float lds[5 * 128 + 10 * 64 + 64 + 64 + 8 * 128 + 16];
  LAS float* qpart = (LAS float*)lds;
  LAS float* hpart = qpart + 5 * 128;
  LAS u32* qb = (LAS u32*)(hpart + 10 * 64);
  LAS u32* hb = qb + 64;
  LAS float* ow = (LAS float*)(hb + 64);
  LAS float* ml = ow + 8 * 128;

  const int tid = threadIdx.x, lane = tid & 63;
  const int wave = __builtin_amdgcn_readfirstlane(tid >> 6);
  const int b = blockIdx.x, h = (b & 7) * 4 + (b >> 6), j = (b >> 3) & 7;
  const int c = lane & 15, g = lane >> 4;
  const bool heavy = wave < 5;
  const bool ffn = h < 27;
  ST_DECL;
  ST(0);

  const __amdgpu_buffer_rsrc_t rx = RSRC(p.x, D_ * 2);
  const __amdgpu_buffer_rsrc_t rwq = RSRC(p.wq, heavy ? D_ * 4096 * 2 : 0);
  const __amdgpu_buffer_rsrc_t rw1 = RSRC(p.w1, (heavy && ffn) ? D_ * FF * 2 : 0);
  const __amdgpu_buffer_rsrc_t rk = RSRC(p.kc, NH * SK * DH * 2);
  const __amdgpu_buffer_rsrc_t rv = RSRC(p.vc, NH * SK * DH * 2);
  const __amdgpu_buffer_rsrc_t rwo = RSRC(p.wo, heavy ? 4096 * D_ * 2 : 0);
  const __amdgpu_buffer_rsrc_t rw2 = RSRC(p.w2, (heavy && ffn) ? FF * D_ * 2 : 0);
  const __amdgpu_buffer_rsrc_t rws = RSRC(p.ws, WS_END);

  const u64 epoch = __hip_atomic_load((GAS u64*)(p.ws + WS_EPOCH), __ATOMIC_RELAXED, __HIP_MEMORY_SCOPE_AGENT);
  const v4u xa = __builtin_amdgcn_raw_buffer_load_b128(rx, lane * 16, 0, 0);
  const v4u xb = __builtin_amdgcn_raw_buffer_load_b128(rx, 1024 + lane * 16, 0, 0);
  const int row0 = 80 * j + 16 * (heavy ? wave : 0);
  const v2u xo = __builtin_amdgcn_raw_buffer_load_b64(rx, g * 8, row0 * 2, 0);
  const v2u xr = __builtin_amdgcn_raw_buffer_load_b64(rx, 0, (b < 160 ? b : 0) * 8, 0);
  SB();
  v4u aq[4], ah[2];
#pragma unroll
  for (int s = 0; s < 4; ++s) aq[s] = ldb(rwq, (4 * g + s) * 8192 + c * 16, row0 * 8192 + h * 256);
#pragma unroll
  for (int s = 0; s < 2; ++s) ah[s] = ldb(rw1, (4 * g + 2 * (c >> 3) + s) * (FF * 2) + (c & 7) * 16, row0 * (FF * 2) + h * 128);
  SB();
  const u32 kvs = (u32)((h * SK + (SK - WIN) + 128 * j + 16 * wave) * 256);
  const u32 kvo = g * 256 + c * 16;
  v4u kk[4], vv[4];
#pragma unroll
  for (int i = 0; i < 4; ++i) kk[i] = ldb(rk, kvo + i * 1024, kvs);
  SB();
  ST(1);

  const u64 T = epoch + 1;
  Tag tg;
  {
    const u32 tl = __builtin_amdgcn_readfirstlane((u32)T), th = __builtin_amdgcn_readfirstlane((u32)(T >> 32));
    const u64 Tu = ((u64)th << 32) | tl;
    tg.f0 = tl & TAGM; tg.f1 = ((u32)(Tu >> 10) + tl) & TAGM; tg.f2 = ((u32)(Tu >> 20) + tl) & TAGM; tg.f3 = ((u32)(Tu >> 30) + tl) & TAGM;
  }
  v4u msk;
  {
    const u32 half = (c & 1) ? 0xffff0000u : 0x0000ffffu; const int r = (c & 7) >> 1;
    msk.x = r == 0 ? half : 0u; msk.y = r == 1 ? half : 0u; msk.z = r == 2 ? half : 0u; msk.w = r == 3 ? half : 0u;
  }
  u32 xd[4];
  {
    float xv[16];
#pragma unroll
    for (int k = 0; k < 4; ++k) { xv[2 * k] = lo_f(xa[k]); xv[2 * k + 1] = hi_f(xa[k]); xv[8 + 2 * k] = lo_f(xb[k]); xv[9 + 2 * k] = hi_f(xb[k]); }
    float s = 0.f;
#pragma unroll
    for (int k = 0; k < 16; ++k) s += xv[k];
    const float mean = xrow_sum(row_sum16(s)) * (1.f / D_);
    float v1 = 0.f, v2 = 0.f;
#pragma unroll
    for (int k = 0; k < 8; ++k) { const float d1 = xv[k] - mean, d2 = xv[8 + k] - mean; v1 = fmaf(d1, d1, v1); v2 = fmaf(d2, d2, v2); }
    const float var = xrow_sum(row_sum16(lane < 16 ? v1 + v2 : v1)) * (1.f / D_);
    const float mb = rbf(mean), den = rbf(sqrtf(rbf(rbf(var) + 1e-5f)));
    const float o0 = rbf(lo_f(xo.x) - mb) / den, o1 = rbf(hi_f(xo.x) - mb) / den, o2 = rbf(lo_f(xo.y) - mb) / den, o3 = rbf(hi_f(xo.y) - mb) / den;
    xd[0] = pk_bf16(o0, o0); xd[1] = pk_bf16(o1, o1); xd[2] = pk_bf16(o2, o2); xd[3] = pk_bf16(o3, o3);
  }
  ST(2);
  {
    f32x4 dq = {0.f, 0.f, 0.f, 0.f}, dh = {0.f, 0.f, 0.f, 0.f};
#pragma unroll
    for (int s = 0; s < 4; ++s) dq = mfma(amask(xd[s], msk), aq[s], dq);
    const u32 h0 = (c >> 3) ? xd[2] : xd[0], h1 = (c >> 3) ? xd[3] : xd[1];
    dh = mfma(amask(h0, msk), ah[0], dh);
    dh = mfma(amask(h1, msk), ah[1], dh);
    if (heavy) {
      if (lane < 32) *(LAS f32x4*)(qpart + wave * 128 + 8 * c + 4 * g) = dq;
      if ((c >> 3) == (g >> 1)) *(LAS f32x4*)(hpart + (2 * wave + (g >> 1)) * 64 + 8 * (c & 7) + 4 * (g & 1)) = dh;
    }
  }
  ST(3);
  BAR();
  if (wave == 7) {
    f32x4 a = {0.f, 0.f, 0.f, 0.f};
    if (lane < 32) {
#pragma unroll
      for (int w = 0; w < 5; ++w) a += *(LAS f32x4*)(qpart + w * 128 + 4 * lane);
    } else if (lane < 48) {
#pragma unroll
      for (int w = 0; w < 10; ++w) a += *(LAS f32x4*)(hpart + w * 64 + 4 * (lane - 32));
    }
    if (lane < 48) stc(tag_pack(a, tg), rws, lane * 16, WS_Q + (h * 8 + j) * 768);
  }
  BAR();
  ST(4);
#pragma unroll
  for (int i = 0; i < 4; ++i) vv[i] = ldb(rv, kvo + i * 1024, kvs);
  SB();
  asm volatile("" ::"v"(kk[0]), "v"(kk[1]), "v"(kk[2]), "v"(kk[3]));
  SB();
  ST(5);
  const int gi = 6 * wave + ((lane >> 3) < 6 ? (lane >> 3) : 5);
  const u32 qoff = (lane & 7) * 768 + gi * 16;
  v4u gq = ldc(rws, qoff, WS_Q + h * 8 * 768);
  SB();
  v4u bo[4], b2[2];
#pragma unroll
  for (int s = 0; s < 4; ++s) bo[s] = ldb(rwo, (4 * g + s) * 1280 + c * 16, (h * 128 + 16 * j) * 1280 + wave * 256);
#pragma unroll
  for (int s = 0; s < 2; ++s) b2[s] = ldb(rw2, (2 * g + s) * 1280 + c * 16, (h * 64 + 8 * j) * 1280 + wave * 256);
  SB();
  if (__builtin_expect(any_lane(tag_bad(gq, tg)), 0)) {
    int spin = 0;
    do { MEMFENCE(); gq = ldc(rws, qoff, WS_Q + h * 8 * 768); } while (any_lane(tag_bad(gq, tg)) && ++spin < SPIN_CAP);
  }
  {
    f32x4 v = tag_strip(gq);
    v.x = oct_sum(v.x); v.y = oct_sum(v.y); v.z = oct_sum(v.z); v.w = oct_sum(v.w);
    if ((lane & 7) == 0 && lane < 48) {
      if (gi < 32) { v2u q2 = {pk_bf16(v.x, v.y), pk_bf16(v.z, v.w)}; *(LAS v2u*)(qb + 2 * gi) = q2; }
      else {
        v4u hv;
#pragma unroll
        for (int k = 0; k < 4; ++k) { const float t = rbf(v[k]); const float sl = t / (1.f + __expf(-t)); hv[k] = pk_bf16(sl, sl); }
        *(LAS v4u*)(hb + 4 * (gi - 32)) = hv;
      }
    }
  }
  ST(6);
  BAR();
  {
    const v4u qf = *(LAS v4u*)(qb + 4 * c);
    float s[4];
#pragma unroll
    for (int i = 0; i < 4; ++i) {
      float d = dot2(kk[i].x, qf.x, 0.f); d = dot2(kk[i].y, qf.y, d); d = dot2(kk[i].z, qf.z, d); d = dot2(kk[i].w, qf.w, d);
      s[i] = rbf(rbf(row_sum16(d)) * 0.08838834764831845f);
    }
    const float m = xrow_max(fmaxf(fmaxf(s[0], s[1]), fmaxf(s[2], s[3])));
    float l = 0.f; f32x4 o = {0.f, 0.f, 0.f, 0.f};
#pragma unroll
    for (int i = 0; i < 4; ++i) { const float pe = __builtin_amdgcn_exp2f((s[i] - m) * LOG2E); l += pe; o = mfma(amask(pk_bf16(pe, pe), msk), vv[i], o); }
    l = xrow_sum(l);
    if (lane < 32) *(LAS f32x4*)(ow + wave * 128 + 8 * c + 4 * g) = o;
    if (lane == 0) { ml[2 * wave] = m; ml[2 * wave + 1] = l; }
  }
  ST(7);
  BAR();
  {
    const int ww = lane & 7;
    f32x4 o = *(LAS f32x4*)(ow + ww * 128 + 16 * wave + 4 * g);
    const float m = ml[2 * ww], l = ml[2 * ww + 1];
    const float M = oct_max(m), f = __builtin_amdgcn_exp2f((m - M) * LOG2E);
    o.x = oct_sum(o.x * f); o.y = oct_sum(o.y * f); o.z = oct_sum(o.z * f); o.w = oct_sum(o.w * f);
    const float L = oct_sum(l * f);
    const u32 base = WS_A + (h * 8 + j) * 768;
    if (c == 0) stc(tag_pack(o, tg), rws, (4 * wave + g) * 16, base);
    if (wave == 0 && lane == 1) { f32x4 t = {M, L, M, L}; stc(tag_pack(t, tg), rws, 32 * 16, base); }
  }
  ST(8);
  if (heavy) {
    const u32 aoff = (lane & 7) * 768 + (4 * j + g) * 16, moff = (lane & 7) * 768 + 32 * 16, abase = WS_A + h * 8 * 768;
    const v2u hv = *(LAS v2u*)(hb + 8 * j + 2 * g);
    v4u go, gm; int spin = 0;
    do { MEMFENCE(); go = ldc(rws, aoff, abase); gm = ldc(rws, moff, abase); } while (any_lane(tag_bad(go, tg) | tag_bad(gm, tg)) && ++spin < SPIN_CAP);
    ST(9);
    f32x4 o = tag_strip(go); const f32x4 mlv = tag_strip(gm);
    const float M = oct_max(mlv.x), f = __builtin_amdgcn_exp2f((mlv.x - M) * LOG2E);
    const float inv = 1.f / oct_sum(mlv.y * f);
    f32x4 y = {0.f, 0.f, 0.f, 0.f};
    y = mfma(amask(hv.x, msk), b2[0], y);
    y = mfma(amask(hv.y, msk), b2[1], y);
#pragma unroll
    for (int s = 0; s < 4; ++s) { const float t = oct_sum(o[s] * f) * inv; y = mfma(amask(pk_bf16(t, t), msk), bo[s], y); }
    if (lane < 32) stc(tag_pack(y, tg), rws, (32 * wave + 2 * c + g) * 16, WS_Y + b * 2560);
    ST(10);
  } else if (wave == 7 && b < 160) {
    v4u gy[4]; u32 bad;
    int spin = 0;
    do {
      MEMFENCE();
#pragma unroll
      for (int k = 0; k < 4; ++k) gy[k] = ldc(rws, (lane + 64 * k) * 2560, WS_Y + b * 16);
      bad = tag_bad(gy[0], tg) | tag_bad(gy[1], tg) | tag_bad(gy[2], tg) | tag_bad(gy[3], tg);
    } while (any_lane(bad) && ++spin < SPIN_CAP);
    ST(11);
    f32x4 a = (tag_strip(gy[0]) + tag_strip(gy[1])) + (tag_strip(gy[2]) + tag_strip(gy[3]));
    a.x = xrow_sum(row_sum16(a.x)); a.y = xrow_sum(row_sum16(a.y)); a.z = xrow_sum(row_sum16(a.z)); a.w = xrow_sum(row_sum16(a.w));
    if (lane == 0) {
      v2u o = {pk_bf16(a.x + lo_f(xr.x), a.y + hi_f(xr.x)), pk_bf16(a.z + lo_f(xr.y), a.w + hi_f(xr.y))};
      *(GAS v2u*)(p.out + 4 * b) = o;
      if (b == 0) __hip_atomic_store((GAS u64*)(p.ws + WS_EPOCH), T, __ATOMIC_RELAXED, __HIP_MEMORY_SCOPE_AGENT);
    }
    ST(12);
  }
  if (blockIdx.x == 0 && wave == 7) {
    u64 now; asm volatile("s_memrealtime %0\n\ts_waitcnt lgkmcnt(0)" : "=s"(now)::"memory");
    const u64 tgt = now + (u64)AMP_G * (st_[AMP_TO] - st_[AMP_FROM]);
    int guard = 0;
    do { asm volatile("s_memrealtime %0\n\ts_waitcnt lgkmcnt(0)" : "=s"(now)::"memory"); } while (now < tgt && ++guard < 1000000);
  }
}

extern "C" void kernel_launch(void* const* d_in, const int* in_sizes, int n_in, void* d_out, int out_size, void* d_ws, size_t ws_size, hipStream_t stream) {
  Params p;
  p.x = (const u16*)d_in[0]; p.kc = (const u16*)d_in[1]; p.vc = (const u16*)d_in[2]; p.wq = (const u16*)d_in[3]; p.wo = (const u16*)d_in[4];
  p.w1 = (const u16*)d_in[6]; p.w2 = (const u16*)d_in[7]; p.out = (u16*)d_out; p.ws = (unsigned char*)d_ws;
  fused_decode<<<256, 512, 0, stream>>>(p);
}
```

```cpp
#define STAMP
#define AMP_G 0
#define AMP_FROM 0
#define AMP_TO 0
#define AMP_KIND 0
#include <hip/hip_runtime.h>
#include <hip/hip_bf16.h>

typedef unsigned short u16;
typedef unsigned int u32;
typedef unsigned long long u64;
typedef u32 v4u __attribute__((ext_vector_type(4)));
typedef u32 v2u __attribute__((ext_vector_type(2)));
typedef float f32x4 __attribute__((ext_vector_type(4)));
typedef __bf16 bf16x8 __attribute__((ext_vector_type(8)));
typedef __bf16 bf16x2 __attribute__((ext_vector_type(2)));
#define GAS __attribute__((address_space(1)))
#define LAS __attribute__((address_space(3)))

#define AUX_STREAM 2
#define AUX_SC1 16

constexpr int D_ = 640, NH = 32, SK = 4096, DH = 128, WIN = 1024, FF = 1728;
constexpr u32 WS_EPOCH = 0;
constexpr u32 WS_Q = 4096;
constexpr u32 WS_A = WS_Q + 32 * 8 * 48 * 16;
constexpr u32 WS_Y = WS_A + 32 * 8 * 48 * 16;
constexpr u32 WS_END = WS_Y + 256 * 160 * 16;
constexpr u32 WS_STAMP = 32u << 20;
constexpr u32 TAGM = 0x3FFu;
constexpr int SPIN_CAP = 200000;

struct Params {
  const u16* x; const u16* kc; const u16* vc; const u16* wq; const u16* wo; const u16* w1; const u16* w2;
  u16* out; unsigned char* ws;
};

#define SB() __builtin_amdgcn_sched_barrier(0)
#define BAR() do { SB(); asm volatile("s_waitcnt lgkmcnt(0)\n\ts_barrier" ::: "memory"); SB(); } while (0)
#define MEMFENCE() asm volatile("" ::: "memory")

#define NSTAMP 13
#define ST(k) do { SB(); asm volatile("s_memrealtime %0\n\ts_waitcnt lgkmcnt(0)" : "=s"(st_[k])::"memory"); SB(); } while (0)
#define ST_DECL u64 st_[NSTAMP]; _Pragma("unroll") for (int k_ = 0; k_ < NSTAMP; ++k_) st_[k_] = 0
#define ST_FLUSH() do { if (lane == 0) { _Pragma("unroll") for (int k_ = 0; k_ < NSTAMP; ++k_) ((GAS u64*)(p.ws + WS_STAMP))[(blockIdx.x * 8 + wave) * NSTAMP + k_] = st_[k_]; } } while (0)

__device__ __forceinline__ float asf(u32 x) { return __builtin_bit_cast(float, x); }
__device__ __forceinline__ u32 asu(float x) { return __builtin_bit_cast(u32, x); }
template <int CTRL> __device__ __forceinline__ float dpp(float x) {
  return asf(__builtin_amdgcn_mov_dpp(asu(x), CTRL, 0xf, 0xf, true));
}
__device__ __forceinline__ float row_sum16(float x) { x += dpp<0x128>(x); x += dpp<0x124>(x); x += dpp<0x122>(x); x += dpp<0x121>(x); return x; }
__device__ __forceinline__ float oct_sum(float x) { x += dpp<0xB1>(x); x += dpp<0x4E>(x); x += dpp<0x141>(x); return x; }
__device__ __forceinline__ float oct_max(float x) { x = fmaxf(x, dpp<0xB1>(x)); x = fmaxf(x, dpp<0x4E>(x)); x = fmaxf(x, dpp<0x141>(x)); return x; }
__device__ __forceinline__ float xrow_sum(float x) {
  auto s = __builtin_amdgcn_permlane16_swap(asu(x), asu(x), false, false);
  x = asf(s[0]) + asf(s[1]);
  auto t = __builtin_amdgcn_permlane32_swap(asu(x), asu(x), false, false);
  return asf(t[0]) + asf(t[1]);
}
__device__ __forceinline__ float xrow_max(float x) {
  auto s = __builtin_amdgcn_permlane16_swap(asu(x), asu(x), false, false);
  x = fmaxf(asf(s[0]), asf(s[1]));
  auto t = __builtin_amdgcn_permlane32_swap(asu(x), asu(x), false, false);
  return fmaxf(asf(t[0]), asf(t[1]));
}
__device__ __forceinline__ u32 pk_bf16(float lo, float hi) {
  bf16x2 r = {(__bf16)lo, (__bf16)hi};
  return __builtin_bit_cast(u32, r);
}
__device__ __forceinline__ float rbf(float x) { return asf(pk_bf16(x, x) << 16); }
__device__ __forceinline__ float lo_f(u32 w) { return asf(w << 16); }
__device__ __forceinline__ float hi_f(u32 w) { return asf(w & 0xffff0000u); }
__device__ __forceinline__ float dot2(u32 a, u32 b, float c) {
  return __builtin_amdgcn_fdot2_f32_bf16(__builtin_bit_cast(bf16x2, a), __builtin_bit_cast(bf16x2, b), c, false);
}
__device__ __forceinline__ f32x4 mfma(v4u a, v4u b, f32x4 c) {
  return __builtin_amdgcn_mfma_f32_16x16x32_bf16(__builtin_bit_cast(bf16x8, a), __builtin_bit_cast(bf16x8, b), c, 0, 0, 0);
}
struct Tag { u32 f0, f1, f2, f3; };
__device__ __forceinline__ v4u tag_pack(f32x4 v, const Tag& t) {
  v4u r;
  r.x = ((asu(v.x) + 0x200u) & ~TAGM) | t.f0; r.y = ((asu(v.y) + 0x200u) & ~TAGM) | t.f1;
  r.z = ((asu(v.z) + 0x200u) & ~TAGM) | t.f2; r.w = ((asu(v.w) + 0x200u) & ~TAGM) | t.f3;
  return r;
}
__device__ __forceinline__ u32 tag_bad(v4u g, const Tag& t) {
  return ((g.x ^ t.f0) | (g.y ^ t.f1) | (g.z ^ t.f2) | (g.w ^ t.f3)) & TAGM;
}
__device__ __forceinline__ bool any_lane(u32 bad) { return __builtin_amdgcn_ballot_w64(bad != 0) != 0; }
__device__ __forceinline__ f32x4 tag_strip(v4u g) {
  f32x4 r = {asf(g.x & ~TAGM), asf(g.y & ~TAGM), asf(g.z & ~TAGM), asf(g.w & ~TAGM)};
  return r;
}
__device__ __forceinline__ v4u amask(u32 dup, const v4u& m) { v4u r = {dup & m.x, dup & m.y, dup & m.z, dup & m.w}; return r; }
#define RSRC(ptr, bytes) __builtin_amdgcn_make_buffer_rsrc((void*)(ptr), 0, (int)(bytes), 0x00020000)
__device__ __forceinline__ v4u ldb(__amdgpu_buffer_rsrc_t r, u32 voff, u32 soff) { return __builtin_amdgcn_raw_buffer_load_b128(r, voff, soff, AUX_STREAM); }
__device__ __forceinline__ v4u ldc(__amdgpu_buffer_rsrc_t r, u32 voff, u32 soff) { return __builtin_amdgcn_raw_buffer_load_b128(r, voff, soff, AUX_SC1); }
__device__ __forceinline__ void stc(v4u v, __amdgpu_buffer_rsrc_t r, u32 voff, u32 soff) { __builtin_amdgcn_raw_buffer_store_b128(v, r, voff, soff, AUX_SC1); }

constexpr float LOG2E = 1.4426950408889634f;

#define CAS __attribute__((address_space(4)))
__global__ void __launch_bounds__(512, 4) fused_decode(Params p_) {
  u64 tfirst; SB(); asm volatile("s_memrealtime %0\n\ts_waitcnt lgkmcnt(0)" : "=s"(tfirst)::"memory"); SB();
  const CAS Params* pp = (const CAS Params*)__builtin_amdgcn_kernarg_segment_ptr();
  asm volatile("" : "+s"(pp));
  Params p; p.x = pp->x; p.kc = pp->kc; p.vc = pp->vc; p.wq = pp->wq; p.wo = pp->wo; p.w1 = pp->w1; p.w2 = pp->w2; p.out = pp->out; p.ws = pp->ws;
  asm volatile("" : "+s"(p.x), "+s"(p.kc), "+s"(p.vc), "+s"(p.wq), "+s"(p.wo), "+s"(p.w1), "+s"(p.w2), "+s"(p.out), "+s"(p.ws));
  __shared__ __attribute__((aligned(16))) float lds[5 * 128 + 10 * 64 + 64 + 64 + 8 * 128 + 16];
  LAS float* qpart = (LAS float*)lds;
  LAS float* hpart = qpart + 5 * 128;
  LAS u32* qb = (LAS u32*)(hpart + 10 * 64);
  LAS u32* hb = qb + 64;
  LAS float* ow = (LAS float*)(hb + 64);
  LAS float* ml = ow + 8 * 128;

  const int tid = threadIdx.x, lane = tid & 63;
  const int wave = __builtin_amdgcn_readfirstlane(tid >> 6);
  const int b = blockIdx.x, h = (b & 7) * 4 + (b >> 6), j = (b >> 3) & 7;
  const int c = lane & 15, g = lane >> 4;
  const bool heavy = wave < 5;
  const bool ffn = h < 27;
  ST_DECL;
  ST(0);

  const __amdgpu_buffer_rsrc_t rx = RSRC(p.x, D_ * 2);
  const __amdgpu_buffer_rsrc_t rwq = RSRC(p.wq, heavy ? D_ * 4096 * 2 : 0);
  const __amdgpu_buffer_rsrc_t rw1 = RSRC(p.w1, (heavy && ffn) ? D_ * FF * 2 : 0);
  const __amdgpu_buffer_rsrc_t rk = RSRC(p.kc, NH * SK * DH * 2);
  const __amdgpu_buffer_rsrc_t rv = RSRC(p.vc, NH * SK * DH * 2);
  const __amdgpu_buffer_rsrc_t rwo = RSRC(p.wo, heavy ? 4096 * D_ * 2 : 0);
  const __amdgpu_buffer_rsrc_t rw2 = RSRC(p.w2, (heavy && ffn) ? FF * D_ * 2 : 0);
  const __amdgpu_buffer_rsrc_t rws = RSRC(p.ws, WS_END);

  const u64 epoch = __hip_atomic_load((GAS u64*)(p.ws + WS_EPOCH), __ATOMIC_RELAXED, __HIP_MEMORY_SCOPE_AGENT);
  const v4u xa = __builtin_amdgcn_raw_buffer_load_b128(rx, lane * 16, 0, 0);
  const v4u xb = __builtin_amdgcn_raw_buffer_load_b128(rx, 1024 + lane * 16, 0, 0);
  const int row0 = 80 * j + 16 * (heavy ? wave : 0);
  const v2u xo = __builtin_amdgcn_raw_buffer_load_b64(rx, g * 8, row0 * 2, 0);
  const v2u xr = __builtin_amdgcn_raw_buffer_load_b64(rx, 0, (b < 160 ? b : 0) * 8, 0);
  SB();
  v4u aq[4], ah[2];
#pragma unroll
  for (int s = 0; s < 4; ++s) aq[s] = ldb(rwq, (4 * g + s) * 8192 + c * 16, row0 * 8192 + h * 256);
#pragma unroll
  for (int s = 0; s < 2; ++s) ah[s] = ldb(rw1, (4 * g + 2 * (c >> 3) + s) * (FF * 2) + (c & 7) * 16, row0 * (FF * 2) + h * 128);
  SB();
  const u32 kvs = (u32)((h * SK + (SK - WIN) + 128 * j + 16 * wave) * 256);
  const u32 kvo = g * 256 + c * 16;
  v4u kk[4], vv[4];
#pragma unroll
  for (int i = 0; i < 4; ++i) kk[i] = ldb(rk, kvo + i * 1024, kvs);
  SB();
  ST(1);

  if (wave == 7 && lane == 0) __hip_atomic_store((GAS u64*)(p.ws + WS_STAMP) + blockIdx.x, tfirst, __ATOMIC_RELAXED, __HIP_MEMORY_SCOPE_AGENT);
  const u64 T = epoch + 1;
  Tag tg;
  {
    const u32 tl = __builtin_amdgcn_readfirstlane((u32)T), th = __builtin_amdgcn_readfirstlane((u32)(T >> 32));
    const u64 Tu = ((u64)th << 32) | tl;
    tg.f0 = tl & TAGM; tg.f1 = ((u32)(Tu >> 10) + tl) & TAGM; tg.f2 = ((u32)(Tu >> 20) + tl) & TAGM; tg.f3 = ((u32)(Tu >> 30) + tl) & TAGM;
  }
  v4u msk;
  {
    const u32 half = (c & 1) ? 0xffff0000u : 0x0000ffffu; const int r = (c & 7) >> 1;
    msk.x = r == 0 ? half : 0u; msk.y = r == 1 ? half : 0u; msk.z = r == 2 ? half : 0u; msk.w = r == 3 ? half : 0u;
  }
  u32 xd[4];
  {
    float xv[16];
#pragma unroll
    for (int k = 0; k < 4; ++k) { xv[2 * k] = lo_f(xa[k]); xv[2 * k + 1] = hi_f(xa[k]); xv[8 + 2 * k] = lo_f(xb[k]); xv[9 + 2 * k] = hi_f(xb[k]); }
    float s = 0.f;
#pragma unroll
    for (int k = 0; k < 16; ++k) s += xv[k];
    const float mean = xrow_sum(row_sum16(s)) * (1.f / D_);
    float v1 = 0.f, v2 = 0.f;
#pragma unroll
    for (int k = 0; k < 8; ++k) { const float d1 = xv[k] - mean, d2 = xv[8 + k] - mean; v1 = fmaf(d1, d1, v1); v2 = fmaf(d2, d2, v2); }
    const float var = xrow_sum(row_sum16(lane < 16 ? v1 + v2 : v1)) * (1.f / D_);
    const float mb = rbf(mean), den = rbf(sqrtf(rbf(rbf(var) + 1e-5f)));
    const float o0 = rbf(lo_f(xo.x) - mb) / den, o1 = rbf(hi_f(xo.x) - mb) / den, o2 = rbf(lo_f(xo.y) - mb) / den, o3 = rbf(hi_f(xo.y) - mb) / den;
    xd[0] = pk_bf16(o0, o0); xd[1] = pk_bf16(o1, o1); xd[2] = pk_bf16(o2, o2); xd[3] = pk_bf16(o3, o3);
  }
  ST(2);
  {
    f32x4 dq = {0.f, 0.f, 0.f, 0.f}, dh = {0.f, 0.f, 0.f, 0.f};
#pragma unroll
    for (int s = 0; s < 4; ++s) dq = mfma(amask(xd[s], msk), aq[s], dq);
    const u32 h0 = (c >> 3) ? xd[2] : xd[0], h1 = (c >> 3) ? xd[3] : xd[1];
    dh = mfma(amask(h0, msk), ah[0], dh);
    dh = mfma(amask(h1, msk), ah[1], dh);
    if (heavy) {
      if (lane < 32) *(LAS f32x4*)(qpart + wave * 128 + 8 * c + 4 * g) = dq;
      if ((c >> 3) == (g >> 1)) *(LAS f32x4*)(hpart + (2 * wave + (g >> 1)) * 64 + 8 * (c & 7) + 4 * (g & 1)) = dh;
    }
  }
  ST(3);
  BAR();
  if (wave == 7) {
    f32x4 a = {0.f, 0.f, 0.f, 0.f};
    if (lane < 32) {
#pragma unroll
      for (int w = 0; w < 5; ++w) a += *(LAS f32x4*)(qpart + w * 128 + 4 * lane);
    } else if (lane < 48) {
#pragma unroll
      for (int w = 0; w < 10; ++w) a += *(LAS f32x4*)(hpart + w * 64 + 4 * (lane - 32));
    }
    if (lane < 48) stc(tag_pack(a, tg), rws, lane * 16, WS_Q + (h * 8 + j) * 768);
  }
  BAR();
  ST(4);
#pragma unroll
  for (int i = 0; i < 4; ++i) vv[i] = ldb(rv, kvo + i * 1024, kvs);
  SB();
  asm volatile("" ::"v"(kk[0]), "v"(kk[1]), "v"(kk[2]), "v"(kk[3]));
  SB();
  ST(5);
  const int gi = 6 * wave + ((lane >> 3) < 6 ? (lane >> 3) : 5);
  const u32 qoff = (lane & 7) * 768 + gi * 16;
  v4u gq = ldc(rws, qoff, WS_Q + h * 8 * 768);
  SB();
  v4u bo[4], b2[2];
#pragma unroll
  for (int s = 0; s < 4; ++s) bo[s] = ldb(rwo, (4 * g + s) * 1280 + c * 16, (h * 128 + 16 * j) * 1280 + wave * 256);
#pragma unroll
  for (int s = 0; s < 2; ++s) b2[s] = ldb(rw2, (2 * g + s) * 1280 + c * 16, (h * 64 + 8 * j) * 1280 + wave * 256);
  SB();
  if (__builtin_expect(any_lane(tag_bad(gq, tg)), 0)) {
    int spin = 0;
    do { MEMFENCE(); gq = ldc(rws, qoff, WS_Q + h * 8 * 768); } while (any_lane(tag_bad(gq, tg)) && ++spin < SPIN_CAP);
  }
  {
    f32x4 v = tag_strip(gq);
    v.x = oct_sum(v.x); v.y = oct_sum(v.y); v.z = oct_sum(v.z); v.w = oct_sum(v.w);
    if ((lane & 7) == 0 && lane < 48) {
      if (gi < 32) { v2u q2 = {pk_bf16(v.x, v.y), pk_bf16(v.z, v.w)}; *(LAS v2u*)(qb + 2 * gi) = q2; }
      else {
        v4u hv;
#pragma unroll
        for (int k = 0; k < 4; ++k) { const float t = rbf(v[k]); const float sl = t / (1.f + __expf(-t)); hv[k] = pk_bf16(sl, sl); }
        *(LAS v4u*)(hb + 4 * (gi - 32)) = hv;
      }
    }
  }
  ST(6);
  BAR();
  {
    const v4u qf = *(LAS v4u*)(qb + 4 * c);
    float s[4];
#pragma unroll
    for (int i = 0; i < 4; ++i) {
      float d = dot2(kk[i].x, qf.x, 0.f); d = dot2(kk[i].y, qf.y, d); d = dot2(kk[i].z, qf.z, d); d = dot2(kk[i].w, qf.w, d);
      s[i] = rbf(rbf(row_sum16(d)) * 0.08838834764831845f);
    }
    const float m = xrow_max(fmaxf(fmaxf(s[0], s[1]), fmaxf(s[2], s[3])));
    float l = 0.f; f32x4 o = {0.f, 0.f, 0.f, 0.f};
#pragma unroll
    for (int i = 0; i < 4; ++i) { const float pe = __builtin_amdgcn_exp2f((s[i] - m) * LOG2E); l += pe; o = mfma(amask(pk_bf16(pe, pe), msk), vv[i], o); }
    l = xrow_sum(l);
    if (lane < 32) *(LAS f32x4*)(ow + wave * 128 + 8 * c + 4 * g) = o;
    if (lane == 0) { ml[2 * wave] = m; ml[2 * wave + 1] = l; }
  }
  ST(7);
  BAR();
  {
    const int ww = lane & 7;
    f32x4 o = *(LAS f32x4*)(ow + ww * 128 + 16 * wave + 4 * g);
    const float m = ml[2 * ww], l = ml[2 * ww + 1];
    const float M = oct_max(m), f = __builtin_amdgcn_exp2f((m - M) * LOG2E);
    o.x = oct_sum(o.x * f); o.y = oct_sum(o.y * f); o.z = oct_sum(o.z * f); o.w = oct_sum(o.w * f);
    const float L = oct_sum(l * f);
    const u32 base = WS_A + (h * 8 + j) * 768;
    if (c == 0) stc(tag_pack(o, tg), rws, (4 * wave + g) * 16, base);
    if (wave == 0 && lane == 1) { f32x4 t = {M, L, M, L}; stc(tag_pack(t, tg), rws, 32 * 16, base); }
  }
  ST(8);
  if (heavy) {
    const u32 aoff = (lane & 7) * 768 + (4 * j + g) * 16, moff = (lane & 7) * 768 + 32 * 16, abase = WS_A + h * 8 * 768;
    const v2u hv = *(LAS v2u*)(hb + 8 * j + 2 * g);
    v4u go, gm; int spin = 0;
    do { MEMFENCE(); go = ldc(rws, aoff, abase); gm = ldc(rws, moff, abase); } while (any_lane(tag_bad(go, tg) | tag_bad(gm, tg)) && ++spin < SPIN_CAP);
    ST(9);
    f32x4 o = tag_strip(go); const f32x4 mlv = tag_strip(gm);
    const float M = oct_max(mlv.x), f = __builtin_amdgcn_exp2f((mlv.x - M) * LOG2E);
    const float inv = 1.f / oct_sum(mlv.y * f);
    f32x4 y = {0.f, 0.f, 0.f, 0.f};
    y = mfma(amask(hv.x, msk), b2[0], y);
    y = mfma(amask(hv.y, msk), b2[1], y);
#pragma unroll
    for (int s = 0; s < 4; ++s) { const float t = oct_sum(o[s] * f) * inv; y = mfma(amask(pk_bf16(t, t), msk), bo[s], y); }
    if (lane < 32) stc(tag_pack(y, tg), rws, (32 * wave + 2 * c + g) * 16, WS_Y + b * 2560);
    ST(10);
  } else if (wave == 7 && b < 160) {
    v4u gy[4]; u32 bad;
    int spin = 0;
    do {
      MEMFENCE();
#pragma unroll
      for (int k = 0; k < 4; ++k) gy[k] = ldc(rws, (lane + 64 * k) * 2560, WS_Y + b * 16);
      bad = tag_bad(gy[0], tg) | tag_bad(gy[1], tg) | tag_bad(gy[2], tg) | tag_bad(gy[3], tg);
    } while (any_lane(bad) && ++spin < SPIN_CAP);
    ST(11);
    f32x4 a = (tag_strip(gy[0]) + tag_strip(gy[1])) + (tag_strip(gy[2]) + tag_strip(gy[3]));
    a.x = xrow_sum(row_sum16(a.x)); a.y = xrow_sum(row_sum16(a.y)); a.z = xrow_sum(row_sum16(a.z)); a.w = xrow_sum(row_sum16(a.w));
    if (lane == 0) {
      v2u o = {pk_bf16(a.x + lo_f(xr.x), a.y + hi_f(xr.x)), pk_bf16(a.z + lo_f(xr.y), a.w + hi_f(xr.y))};
      *(GAS v2u*)(p.out + 4 * b) = o;
      if (b == 0) __hip_atomic_store((GAS u64*)(p.ws + WS_EPOCH), T, __ATOMIC_RELAXED, __HIP_MEMORY_SCOPE_AGENT);
    }
    ST(12);
  }
  if (blockIdx.x == 0 && wave == 7) {
    u64 now; asm volatile("s_memrealtime %0\n\ts_waitcnt lgkmcnt(0)" : "=s"(now)::"memory");
    const u64 iv = st_[AMP_TO] - st_[AMP_FROM];
    asm volatile("s_memrealtime %0\n\ts_waitcnt lgkmcnt(0)" : "=s"(now)::"memory");
    const u64 tgt = now + (u64)AMP_G * (iv > 100000 ? 0 : iv);
    int guard = 0;
    do { asm volatile("s_memrealtime %0\n\ts_waitcnt lgkmcnt(0)" : "=s"(now)::"memory"); } while (now < tgt && ++guard < 1000000);
  }
}

extern "C" void kernel_launch(void* const* d_in, const int* in_sizes, int n_in, void* d_out, int out_size, void* d_ws, size_t ws_size, hipStream_t stream) {
  Params p;
  p.x = (const u16*)d_in[0]; p.kc = (const u16*)d_in[1]; p.vc = (const u16*)d_in[2]; p.wq = (const u16*)d_in[3]; p.wo = (const u16*)d_in[4];
  p.w1 = (const u16*)d_in[6]; p.w2 = (const u16*)d_in[7]; p.out = (u16*)d_out; p.ws = (unsigned char*)d_ws;
  fused_decode<<<256, 512, 0, stream>>>(p);
}
```

```cpp
#include <hip/hip_runtime.h>
#include <hip/hip_bf16.h>

typedef unsigned short u16;
typedef unsigned int u32;
typedef unsigned long long u64;
typedef u32 v4u __attribute__((ext_vector_type(4)));
typedef u32 v2u __attribute__((ext_vector_type(2)));
typedef float f32x4 __attribute__((ext_vector_type(4)));
typedef __bf16 bf16x8 __attribute__((ext_vector_type(8)));
typedef __bf16 bf16x2 __attribute__((ext_vector_type(2)));
#define GAS __attribute__((address_space(1)))
#define LAS __attribute__((address_space(3)))

#define AUX_STREAM 2
#define AUX_SC1 16

constexpr int D_ = 640, NH = 32, SK = 4096, DH = 128, WIN = 1024, FF = 1728;
constexpr u32 WS_EPOCH = 0;
constexpr u32 WS_Q = 4096;
constexpr u32 WS_A = WS_Q + 32 * 8 * 48 * 16;
constexpr u32 WS_Y = WS_A + 32 * 8 * 48 * 16;
constexpr u32 WS_END = WS_Y + 256 * 160 * 16;
constexpr u32 WS_STAMP = 32u << 20;
constexpr u32 TAGM = 0x3FFu;
constexpr int SPIN_CAP = 200000;

struct Params {
  const u16* x; const u16* kc; const u16* vc; const u16* wq; const u16* wo; const u16* w1; const u16* w2;
  u16* out; unsigned char* ws;
};

#define SB() __builtin_amdgcn_sched_barrier(0)
#define BAR() do { SB(); asm volatile("s_waitcnt lgkmcnt(0)\n\ts_barrier" ::: "memory"); SB(); } while (0)
#define MEMFENCE() asm volatile("" ::: "memory")

#define ST(k)
#define ST_DECL
#define ST_FLUSH()

__device__ __forceinline__ float asf(u32 x) { return __builtin_bit_cast(float, x); }
__device__ __forceinline__ u32 asu(float x) { return __builtin_bit_cast(u32, x); }
template <int CTRL> __device__ __forceinline__ float dpp(float x) {
  return asf(__builtin_amdgcn_mov_dpp(asu(x), CTRL, 0xf, 0xf, true));
}
__device__ __forceinline__ float row_sum16(float x) { x += dpp<0x128>(x); x += dpp<0x124>(x); x += dpp<0x122>(x); x += dpp<0x121>(x); return x; }
__device__ __forceinline__ float oct_sum(float x) { x += dpp<0xB1>(x); x += dpp<0x4E>(x); x += dpp<0x141>(x); return x; }
__device__ __forceinline__ float oct_max(float x) { x = fmaxf(x, dpp<0xB1>(x)); x = fmaxf(x, dpp<0x4E>(x)); x = fmaxf(x, dpp<0x141>(x)); return x; }
__device__ __forceinline__ float xrow_sum(float x) {
  auto s = __builtin_amdgcn_permlane16_swap(asu(x), asu(x), false, false);
  x = asf(s[0]) + asf(s[1]);
  auto t = __builtin_amdgcn_permlane32_swap(asu(x), asu(x), false, false);
  return asf(t[0]) + asf(t[1]);
}
__device__ __forceinline__ float xrow_max(float x) {
  auto s = __builtin_amdgcn_permlane16_swap(asu(x), asu(x), false, false);
  x = fmaxf(asf(s[0]), asf(s[1]));
  auto t = __builtin_amdgcn_permlane32_swap(asu(x), asu(x), false, false);
  return fmaxf(asf(t[0]), asf(t[1]));
}
__device__ __forceinline__ u32 pk_bf16(float lo, float hi) {
  bf16x2 r = {(__bf16)lo, (__bf16)hi};
  return __builtin_bit_cast(u32, r);
}
__device__ __forceinline__ float rbf(float x) { return asf(pk_bf16(x, x) << 16); }
__device__ __forceinline__ float lo_f(u32 w) { return asf(w << 16); }
__device__ __forceinline__ float hi_f(u32 w) { return asf(w & 0xffff0000u); }
__device__ __forceinline__ float dot2(u32 a, u32 b, float c) {
  return __builtin_amdgcn_fdot2_f32_bf16(__builtin_bit_cast(bf16x2, a), __builtin_bit_cast(bf16x2, b), c, false);
}
__device__ __forceinline__ f32x4 mfma(v4u a, v4u b, f32x4 c) {
  return __builtin_amdgcn_mfma_f32_16x16x32_bf16(__builtin_bit_cast(bf16x8, a), __builtin_bit_cast(bf16x8, b), c, 0, 0, 0);
}
struct Tag { u32 f0, f1, f2, f3; };
__device__ __forceinline__ v4u tag_pack(f32x4 v, const Tag& t) {
  v4u r;
  r.x = ((asu(v.x) + 0x200u) & ~TAGM) | t.f0; r.y = ((asu(v.y) + 0x200u) & ~TAGM) | t.f1;
  r.z = ((asu(v.z) + 0x200u) & ~TAGM) | t.f2; r.w = ((asu(v.w) + 0x200u) & ~TAGM) | t.f3;
  return r;
}
__device__ __forceinline__ u32 tag_bad(v4u g, const Tag& t) {
  return ((g.x ^ t.f0) | (g.y ^ t.f1) | (g.z ^ t.f2) | (g.w ^ t.f3)) & TAGM;
}
__device__ __forceinline__ bool any_lane(u32 bad) { return __builtin_amdgcn_ballot_w64(bad != 0) != 0; }
__device__ __forceinline__ f32x4 tag_strip(v4u g) {
  f32x4 r = {asf(g.x & ~TAGM), asf(g.y & ~TAGM), asf(g.z & ~TAGM), asf(g.w & ~TAGM)};
  return r;
}
__device__ __forceinline__ v4u amask(u32 dup, const v4u& m) { v4u r = {dup & m.x, dup & m.y, dup & m.z, dup & m.w}; return r; }
#define RSRC(ptr, bytes) __builtin_amdgcn_make_buffer_rsrc((void*)(ptr), 0, (int)(bytes), 0x00020000)
__device__ __forceinline__ v4u ldb(__amdgpu_buffer_rsrc_t r, u32 voff, u32 soff) { return __builtin_amdgcn_raw_buffer_load_b128(r, voff, soff, AUX_STREAM); }
__device__ __forceinline__ v4u ldc(__amdgpu_buffer_rsrc_t r, u32 voff, u32 soff) { return __builtin_amdgcn_raw_buffer_load_b128(r, voff, soff, AUX_SC1); }
__device__ __forceinline__ void stc(v4u v, __amdgpu_buffer_rsrc_t r, u32 voff, u32 soff) { __builtin_amdgcn_raw_buffer_store_b128(v, r, voff, soff, AUX_SC1); }

constexpr float LOG2E = 1.4426950408889634f;

__global__ void __launch_bounds__(512, 4) fused_decode(Params p) {
  __shared__ __attribute__((aligned(16))) float lds[5 * 128 + 10 * 64 + 64 + 64 + 8 * 128 + 16];
  LAS float* qpart = (LAS float*)lds;
  LAS float* hpart = qpart + 5 * 128;
  LAS u32* qb = (LAS u32*)(hpart + 10 * 64);
  LAS u32* hb = qb + 64;
  LAS float* ow = (LAS float*)(hb + 64);
  LAS float* ml = ow + 8 * 128;

  const int tid = threadIdx.x, lane = tid & 63;
  const int wave = __builtin_amdgcn_readfirstlane(tid >> 6);
  const int b = blockIdx.x, h = (b & 7) * 4 + (b >> 6), j = (b >> 3) & 7;
  const int c = lane & 15, g = lane >> 4;
  const bool heavy = wave < 5;
  const bool ffn = h < 27;
  ST_DECL;
  ST(0);

  const __amdgpu_buffer_rsrc_t rx = RSRC(p.x, D_ * 2);
  const __amdgpu_buffer_rsrc_t rwq = RSRC(p.wq, heavy ? D_ * 4096 * 2 : 0);
  const __amdgpu_buffer_rsrc_t rw1 = RSRC(p.w1, (heavy && ffn) ? D_ * FF * 2 : 0);
  const __amdgpu_buffer_rsrc_t rk = RSRC(p.kc, NH * SK * DH * 2);
  const __amdgpu_buffer_rsrc_t rv = RSRC(p.vc, NH * SK * DH * 2);
  const __amdgpu_buffer_rsrc_t rwo = RSRC(p.wo, heavy ? 4096 * D_ * 2 : 0);
  const __amdgpu_buffer_rsrc_t rw2 = RSRC(p.w2, (heavy && ffn) ? FF * D_ * 2 : 0);
  const __amdgpu_buffer_rsrc_t rws = RSRC(p.ws, WS_END);

  const u64 epoch = __hip_atomic_load((GAS u64*)(p.ws + WS_EPOCH), __ATOMIC_RELAXED, __HIP_MEMORY_SCOPE_AGENT);
  const v4u xa = __builtin_amdgcn_raw_buffer_load_b128(rx, lane * 16, 0, 0);
  const v4u xb = __builtin_amdgcn_raw_buffer_load_b128(rx, 1024 + lane * 16, 0, 0);
  const int row0 = 80 * j + 16 * (heavy ? wave : 0);
  const v2u xo = __builtin_amdgcn_raw_buffer_load_b64(rx, g * 8, row0 * 2, 0);
  const v2u xr = __builtin_amdgcn_raw_buffer_load_b64(rx, 0, (b < 160 ? b : 0) * 8, 0);
  const u32 touch0 = __builtin_amdgcn_raw_buffer_load_b32(RSRC(p.out, 1280), 0, 0, 0);
  const u32 touch1 = __builtin_amdgcn_raw_buffer_load_b32(RSRC(p.vc, NH * SK * DH * 2), 0, (u32)((h * SK + (SK - WIN) + 128 * j) * 256), 0);
  const u32 touch2 = __builtin_amdgcn_raw_buffer_load_b32(RSRC(p.wo, 4096 * D_ * 2), 0, (h * 128 + 16 * j) * 1280, 0);
  const u32 touch3 = __builtin_amdgcn_raw_buffer_load_b32(RSRC(p.w2, FF * D_ * 2), 0, ((h < 27 ? h : 0) * 64 + 8 * j) * 1280, 0);
  SB();
  v4u aq[4], ah[2];
#pragma unroll
  for (int s = 0; s < 4; ++s) aq[s] = ldb(rwq, (4 * g + s) * 8192 + c * 16, row0 * 8192 + h * 256);
#pragma unroll
  for (int s = 0; s < 2; ++s) ah[s] = ldb(rw1, (4 * g + 2 * (c >> 3) + s) * (FF * 2) + (c & 7) * 16, row0 * (FF * 2) + h * 128);
  SB();
  const u32 kvs = (u32)((h * SK + (SK - WIN) + 128 * j + 16 * wave) * 256);
  const u32 kvo = g * 256 + c * 16;
  v4u kk[4], vv[4];
#pragma unroll
  for (int i = 0; i < 4; ++i) kk[i] = ldb(rk, kvo + i * 1024, kvs);
  SB();
  ST(1);

  const u64 T = epoch + 1;
  Tag tg;
  {
    const u32 tl = __builtin_amdgcn_readfirstlane((u32)T), th = __builtin_amdgcn_readfirstlane((u32)(T >> 32));
    const u64 Tu = ((u64)th << 32) | tl;
    tg.f0 = tl & TAGM; tg.f1 = ((u32)(Tu >> 10) + tl) & TAGM; tg.f2 = ((u32)(Tu >> 20) + tl) & TAGM; tg.f3 = ((u32)(Tu >> 30) + tl) & TAGM;
  }
  v4u msk;
  {
    const u32 half = (c & 1) ? 0xffff0000u : 0x0000ffffu; const int r = (c & 7) >> 1;
    msk.x = r == 0 ? half : 0u; msk.y = r == 1 ? half : 0u; msk.z = r == 2 ? half : 0u; msk.w = r == 3 ? half : 0u;
  }
  u32 xd[4];
  {
    float xv[16];
#pragma unroll
    for (int k = 0; k < 4; ++k) { xv[2 * k] = lo_f(xa[k]); xv[2 * k + 1] = hi_f(xa[k]); xv[8 + 2 * k] = lo_f(xb[k]); xv[9 + 2 * k] = hi_f(xb[k]); }
    float s = 0.f;
#pragma unroll
    for (int k = 0; k < 16; ++k) s += xv[k];
    const float mean = xrow_sum(row_sum16(s)) * (1.f / D_);
    float v1 = 0.f, v2 = 0.f;
#pragma unroll
    for (int k = 0; k < 8; ++k) { const float d1 = xv[k] - mean, d2 = xv[8 + k] - mean; v1 = fmaf(d1, d1, v1); v2 = fmaf(d2, d2, v2); }
    const float var = xrow_sum(row_sum16(lane < 16 ? v1 + v2 : v1)) * (1.f / D_);
    const float mb = rbf(mean), den = rbf(sqrtf(rbf(rbf(var) + 1e-5f)));
    const float o0 = rbf(lo_f(xo.x) - mb) / den, o1 = rbf(hi_f(xo.x) - mb) / den, o2 = rbf(lo_f(xo.y) - mb) / den, o3 = rbf(hi_f(xo.y) - mb) / den;
    xd[0] = pk_bf16(o0, o0); xd[1] = pk_bf16(o1, o1); xd[2] = pk_bf16(o2, o2); xd[3] = pk_bf16(o3, o3);
  }
  asm volatile("" :: "v"(touch0), "v"(touch1), "v"(touch2), "v"(touch3));
  ST(2);
  {
    f32x4 dq = {0.f, 0.f, 0.f, 0.f}, dh = {0.f, 0.f, 0.f, 0.f};
#pragma unroll
    for (int s = 0; s < 4; ++s) dq = mfma(amask(xd[s], msk), aq[s], dq);
    const u32 h0 = (c >> 3) ? xd[2] : xd[0], h1 = (c >> 3) ? xd[3] : xd[1];
    dh = mfma(amask(h0, msk), ah[0], dh);
    dh = mfma(amask(h1, msk), ah[1], dh);
    if (heavy) {
      if (lane < 32) *(LAS f32x4*)(qpart + wave * 128 + 8 * c + 4 * g) = dq;
      if ((c >> 3) == (g >> 1)) *(LAS f32x4*)(hpart + (2 * wave + (g >> 1)) * 64 + 8 * (c & 7) + 4 * (g & 1)) = dh;
    }
  }
  ST(3);
  BAR();
  if (wave == 7) {
    f32x4 a = {0.f, 0.f, 0.f, 0.f};
    if (lane < 32) {
#pragma unroll
      for (int w = 0; w < 5; ++w) a += *(LAS f32x4*)(qpart + w * 128 + 4 * lane);
    } else if (lane < 48) {
#pragma unroll
      for (int w = 0; w < 10; ++w) a += *(LAS f32x4*)(hpart + w * 64 + 4 * (lane - 32));
    }
    if (lane < 48) stc(tag_pack(a, tg), rws, lane * 16, WS_Q + (h * 8 + j) * 768);
  }
  BAR();
  ST(4);
#pragma unroll
  for (int i = 0; i < 4; ++i) vv[i] = ldb(rv, kvo + i * 1024, kvs);
  SB();
  asm volatile("" ::"v"(kk[0]), "v"(kk[1]), "v"(kk[2]), "v"(kk[3]));
  SB();
  ST(5);
  const int gi = 6 * wave + ((lane >> 3) < 6 ? (lane >> 3) : 5);
  const u32 qoff = (lane & 7) * 768 + gi * 16;
  v4u gq = ldc(rws, qoff, WS_Q + h * 8 * 768);
  SB();
  v4u bo[4], b2[2];
#pragma unroll
  for (int s = 0; s < 4; ++s) bo[s] = ldb(rwo, (4 * g + s) * 1280 + c * 16, (h * 128 + 16 * j) * 1280 + wave * 256);
#pragma unroll
  for (int s = 0; s < 2; ++s) b2[s] = ldb(rw2, (2 * g + s) * 1280 + c * 16, (h * 64 + 8 * j) * 1280 + wave * 256);
  SB();
  if (__builtin_expect(any_lane(tag_bad(gq, tg)), 0)) {
    int spin = 0;
    do { MEMFENCE(); gq = ldc(rws, qoff, WS_Q + h * 8 * 768); } while (any_lane(tag_bad(gq, tg)) && ++spin < SPIN_CAP);
  }
  {
    f32x4 v = tag_strip(gq);
    v.x = oct_sum(v.x); v.y = oct_sum(v.y); v.z = oct_sum(v.z); v.w = oct_sum(v.w);
    if ((lane & 7) == 0 && lane < 48) {
      if (gi < 32) { v2u q2 = {pk_bf16(v.x, v.y), pk_bf16(v.z, v.w)}; *(LAS v2u*)(qb + 2 * gi) = q2; }
      else {
        v4u hv;
#pragma unroll
        for (int k = 0; k < 4; ++k) { const float t = rbf(v[k]); const float sl = t / (1.f + __expf(-t)); hv[k] = pk_bf16(sl, sl); }
        *(LAS v4u*)(hb + 4 * (gi - 32)) = hv;
      }
    }
  }
  ST(6);
  BAR();
  {
    const v4u qf = *(LAS v4u*)(qb + 4 * c);
    float s[4];
#pragma unroll
    for (int i = 0; i < 4; ++i) {
      float d = dot2(kk[i].x, qf.x, 0.f); d = dot2(kk[i].y, qf.y, d); d = dot2(kk[i].z, qf.z, d); d = dot2(kk[i].w, qf.w, d);
      s[i] = rbf(rbf(row_sum16(d)) * 0.08838834764831845f);
    }
    const float m = xrow_max(fmaxf(fmaxf(s[0], s[1]), fmaxf(s[2], s[3])));
    float l = 0.f; f32x4 o = {0.f, 0.f, 0.f, 0.f};
#pragma unroll
    for (int i = 0; i < 4; ++i) { const float pe = __builtin_amdgcn_exp2f((s[i] - m) * LOG2E); l += pe; o = mfma(amask(pk_bf16(pe, pe), msk), vv[i], o); }
    l = xrow_sum(l);
    if (lane < 32) *(LAS f32x4*)(ow + wave * 128 + 8 * c + 4 * g) = o;
    if (lane == 0) { ml[2 * wave] = m; ml[2 * wave + 1] = l; }
  }
  ST(7);
  BAR();
  {
    const int ww = lane & 7;
    f32x4 o = *(LAS f32x4*)(ow + ww * 128 + 16 * wave + 4 * g);
    const float m = ml[2 * ww], l = ml[2 * ww + 1];
    const float M = oct_max(m), f = __builtin_amdgcn_exp2f((m - M) * LOG2E);
    o.x = oct_sum(o.x * f); o.y = oct_sum(o.y * f); o.z = oct_sum(o.z * f); o.w = oct_sum(o.w * f);
    const float L = oct_sum(l * f);
    const u32 base = WS_A + (h * 8 + j) * 768;
    if (c == 0) stc(tag_pack(o, tg), rws, (4 * wave + g) * 16, base);
    if (wave == 0 && lane == 1) { f32x4 t = {M, L, M, L}; stc(tag_pack(t, tg), rws, 32 * 16, base); }
  }
  ST(8);
  if (heavy) {
    const u32 aoff = (lane & 7) * 768 + (4 * j + g) * 16, moff = (lane & 7) * 768 + 32 * 16, abase = WS_A + h * 8 * 768;
    const v2u hv = *(LAS v2u*)(hb + 8 * j + 2 * g);
    v4u go, gm; int spin = 0;
    do { MEMFENCE(); go = ldc(rws, aoff, abase); gm = ldc(rws, moff, abase); } while (any_lane(tag_bad(go, tg) | tag_bad(gm, tg)) && ++spin < SPIN_CAP);
    ST(9);
    f32x4 o = tag_strip(go); const f32x4 mlv = tag_strip(gm);
    const float M = oct_max(mlv.x), f = __builtin_amdgcn_exp2f((mlv.x - M) * LOG2E);
    const float inv = 1.f / oct_sum(mlv.y * f);
    f32x4 y = {0.f, 0.f, 0.f, 0.f};
    y = mfma(amask(hv.x, msk), b2[0], y);
    y = mfma(amask(hv.y, msk), b2[1], y);
#pragma unroll
    for (int s = 0; s < 4; ++s) { const float t = oct_sum(o[s] * f) * inv; y = mfma(amask(pk_bf16(t, t), msk), bo[s], y); }
    if (lane < 32) stc(tag_pack(y, tg), rws, (32 * wave + 2 * c + g) * 16, WS_Y + b * 2560);
    ST(10);
  } else if (wave == 7 && b < 160) {
    v4u gy[4]; u32 bad;
    int spin = 0;
    do {
      MEMFENCE();
#pragma unroll
      for (int k = 0; k < 4; ++k) gy[k] = ldc(rws, (lane + 64 * k) * 2560, WS_Y + b * 16);
      bad = tag_bad(gy[0], tg) | tag_bad(gy[1], tg) | tag_bad(gy[2], tg) | tag_bad(gy[3], tg);
    } while (any_lane(bad) && ++spin < SPIN_CAP);
    ST(11);
    f32x4 a = (tag_strip(gy[0]) + tag_strip(gy[1])) + (tag_strip(gy[2]) + tag_strip(gy[3]));
    a.x = xrow_sum(row_sum16(a.x)); a.y = xrow_sum(row_sum16(a.y)); a.z = xrow_sum(row_sum16(a.z)); a.w = xrow_sum(row_sum16(a.w));
    if (lane == 0) {
      v2u o = {pk_bf16(a.x + lo_f(xr.x), a.y + hi_f(xr.x)), pk_bf16(a.z + lo_f(xr.y), a.w + hi_f(xr.y))};
      *(GAS v2u*)(p.out + 4 * b) = o;
      if (b == 0) __hip_atomic_store((GAS u64*)(p.ws + WS_EPOCH), T, __ATOMIC_RELAXED, __HIP_MEMORY_SCOPE_AGENT);
    }
    ST(12);
  }
  ST_FLUSH();
}

extern "C" void kernel_launch(void* const* d_in, const int* in_sizes, int n_in, void* d_out, int out_size, void* d_ws, size_t ws_size, hipStream_t stream) {
  Params p;
  p.x = (const u16*)d_in[0]; p.kc = (const u16*)d_in[1]; p.vc = (const u16*)d_in[2]; p.wq = (const u16*)d_in[3]; p.wo = (const u16*)d_in[4];
  p.w1 = (const u16*)d_in[6]; p.w2 = (const u16*)d_in[7]; p.out = (u16*)d_out; p.ws = (unsigned char*)d_ws;
  fused_decode<<<256, 512, 0, stream>>>(p);
}
```

```cpp
#include <hip/hip_runtime.h>
#include <hip/hip_bf16.h>

typedef unsigned short u16;
typedef unsigned int u32;
typedef unsigned long long u64;
typedef u32 v4u __attribute__((ext_vector_type(4)));
typedef u32 v2u __attribute__((ext_vector_type(2)));
typedef float f32x4 __attribute__((ext_vector_type(4)));
typedef __bf16 bf16x8 __attribute__((ext_vector_type(8)));
typedef __bf16 bf16x2 __attribute__((ext_vector_type(2)));
#define GAS __attribute__((address_space(1)))
#define LAS __attribute__((address_space(3)))

#define KVWIN 6
#define QDELAY 28
#define ADELAY 20
#define FDELAY 28
#define B0POS 0
#define QSPIN 1
constexpr int AUX_NT = 2, AUX_SC1 = 16;

constexpr int D_ = 640, NH = 32, SK = 4096, DH = 128, WIN = 1024, FF = 1728;
constexpr u32 WQ_P = 512, WH_P = 256, WA_P = 640;
constexpr u32 WS_EPOCH = 0;
constexpr u32 WS_Q = 4096;
constexpr u32 WS_H = WS_Q + 32 * 16 * WQ_P;
constexpr u32 WS_A = WS_H + 32 * 16 * WH_P;
constexpr u32 WS_Y = WS_A + 32 * 32 * WA_P;
constexpr u32 WS_END = WS_Y + 256 * 160 * 16;
constexpr u32 WS_STAMP = 32u << 20;
constexpr u32 TAGM = 0x3FFu;
constexpr int SPIN_CAP = 200000;
constexpr float LOG2E = 1.4426950408889634f;

struct Params {
  const u16* x; const u16* wq; unsigned char* ws; const u16* kc; const u16* vc; const u16* w1; const u16* wo; const u16* w2; u16* out;
};

#define SB() __builtin_amdgcn_sched_barrier(0)
#define BAR() do { SB(); asm volatile("s_waitcnt lgkmcnt(0)\n\ts_barrier" ::: "memory"); SB(); } while (0)
#define MEMFENCE() asm volatile("" ::: "memory")

#define ST(k)
#define ST_DECL
#define ST_FLUSH()

__device__ __forceinline__ float asf(u32 x) { return __builtin_bit_cast(float, x); }
__device__ __forceinline__ u32 asu(float x) { return __builtin_bit_cast(u32, x); }
constexpr int ROR8 = 0x128, ROR4 = 0x124, ROR2 = 0x122, ROR1 = 0x121, HMIR = 0x141, XOR2 = 0x4E, XOR1 = 0xB1;
template <int CTRL> __device__ __forceinline__ float dpp(float x) { return asf(__builtin_amdgcn_mov_dpp(asu(x), CTRL, 0xf, 0xf, true)); }
template <int L> __device__ __forceinline__ u32 row_bcast(u32 x) { return __builtin_amdgcn_mov_dpp(x, 0x150 + L, 0xf, 0xf, true); }
__device__ __forceinline__ float row_sum16(float x) { x += dpp<ROR8>(x); x += dpp<ROR4>(x); x += dpp<ROR2>(x); x += dpp<ROR1>(x); return x; }
__device__ __forceinline__ float vmax(float a, float b) { float r; asm("v_max_f32 %0, %1, %2" : "=v"(r) : "v"(a), "v"(b)); return r; }
#define DPP_MAX(name, ctrl) __device__ __forceinline__ float name(float x) { float r; asm("s_nop 1\n\tv_max_f32_dpp %0, %1, %1 " ctrl " row_mask:0xf bank_mask:0xf" : "=&v"(r) : "v"(x)); return r; }
DPP_MAX(max_ror8, "row_ror:8") DPP_MAX(max_ror4, "row_ror:4") DPP_MAX(max_ror2, "row_ror:2") DPP_MAX(max_ror1, "row_ror:1")
DPP_MAX(max_hmir, "row_half_mirror") DPP_MAX(max_xor2, "quad_perm:[2,3,0,1]") DPP_MAX(max_xor1, "quad_perm:[1,0,3,2]")
__device__ __forceinline__ float xrow_sum(float x) {
  auto s = __builtin_amdgcn_permlane16_swap(asu(x), asu(x), false, false);
  x = asf(s[0]) + asf(s[1]);
  auto t = __builtin_amdgcn_permlane32_swap(asu(x), asu(x), false, false);
  return asf(t[0]) + asf(t[1]);
}
__device__ __forceinline__ float xrow_max(float x) {
  auto s = __builtin_amdgcn_permlane16_swap(asu(x), asu(x), false, false);
  x = vmax(asf(s[0]), asf(s[1]));
  auto t = __builtin_amdgcn_permlane32_swap(asu(x), asu(x), false, false);
  return vmax(asf(t[0]), asf(t[1]));
}
__device__ __forceinline__ float xrow_sum4(f32x4 a) {
  auto s0 = __builtin_amdgcn_permlane32_swap(asu(a.x), asu(a.z), false, false);
  auto s1 = __builtin_amdgcn_permlane32_swap(asu(a.y), asu(a.w), false, false);
  auto s2 = __builtin_amdgcn_permlane16_swap(asu(asf(s0[0]) + asf(s0[1])), asu(asf(s1[0]) + asf(s1[1])), false, false);
  return asf(s2[0]) + asf(s2[1]);
}
__device__ __forceinline__ u32 pk_bf16(float lo, float hi) {
  bf16x2 r = {(__bf16)lo, (__bf16)hi};
  return __builtin_bit_cast(u32, r);
}
__device__ __forceinline__ float rbf(float x) { return asf(pk_bf16(x, x) << 16); }
__device__ __forceinline__ float lo_f(u32 w) { return asf(w << 16); }
__device__ __forceinline__ float hi_f(u32 w) { return asf(w & 0xffff0000u); }
__device__ __forceinline__ float rcp(float x) { return __builtin_amdgcn_rcpf(x); }
__device__ __forceinline__ float ex2(float x) { return __builtin_amdgcn_exp2f(x); }
__device__ __forceinline__ float dot2(u32 a, u32 b, float c) {
  return __builtin_amdgcn_fdot2_f32_bf16(__builtin_bit_cast(bf16x2, a), __builtin_bit_cast(bf16x2, b), c, false);
}
__device__ __forceinline__ f32x4 mfma(v4u a, v4u b, f32x4 c) {
  return __builtin_amdgcn_mfma_f32_16x16x32_bf16(__builtin_bit_cast(bf16x8, a), __builtin_bit_cast(bf16x8, b), c, 0, 0, 0);
}
__device__ __forceinline__ v4u amask(u32 dup, const v4u& m) { v4u r = {dup & m.x, dup & m.y, dup & m.z, dup & m.w}; return r; }

struct Tag { u32 f0, f1, f2, f3, keep; };
__device__ __forceinline__ Tag make_tag(u64 T) {
  const u32 tl = __builtin_amdgcn_readfirstlane((u32)T), th = __builtin_amdgcn_readfirstlane((u32)(T >> 32));
  const u64 Tu = ((u64)th << 32) | tl;
  Tag tg; tg.f0 = tl & TAGM; tg.f1 = ((u32)(Tu >> 10) + tl) & TAGM; tg.f2 = ((u32)(Tu >> 20) + tl) & TAGM; tg.f3 = ((u32)(Tu >> 30) + tl) & TAGM;
  tg.keep = ~TAGM;
  asm volatile("" : "+s"(tg.f0), "+s"(tg.f1), "+s"(tg.f2), "+s"(tg.f3), "+v"(tg.keep));
  return tg;
}
__device__ __forceinline__ v4u tag_pack(f32x4 v, const Tag& t) {
  v4u r = {(asu(v.x) & t.keep) | t.f0, (asu(v.y) & t.keep) | t.f1, (asu(v.z) & t.keep) | t.f2, (asu(v.w) & t.keep) | t.f3};
  return r;
}
__device__ __forceinline__ u32 tag_acc(u32 acc, v4u g, const Tag& t) {
  acc = __builtin_amdgcn_bitop3_b32(g.x, t.f0, acc, 0xBE); acc = __builtin_amdgcn_bitop3_b32(g.y, t.f1, acc, 0xBE);
  acc = __builtin_amdgcn_bitop3_b32(g.z, t.f2, acc, 0xBE); acc = __builtin_amdgcn_bitop3_b32(g.w, t.f3, acc, 0xBE);
  return acc;
}
__device__ __forceinline__ bool any_bad(u32 acc) { return __builtin_amdgcn_ballot_w64((acc & TAGM) != 0) != 0; }
__device__ __forceinline__ float untag(u32 w) { return asf(w & ~TAGM); }
__device__ __forceinline__ f32x4 untag4(v4u g) { f32x4 r = {untag(g.x), untag(g.y), untag(g.z), untag(g.w)}; return r; }

#define RSRC(ptr, bytes) __builtin_amdgcn_make_buffer_rsrc((void*)(ptr), 0, (int)(bytes), 0x00020000)
__device__ __forceinline__ v4u ld_stream(__amdgpu_buffer_rsrc_t r, u32 voff, u32 soff) { return __builtin_amdgcn_raw_buffer_load_b128(r, voff, soff, AUX_NT); }
__device__ __forceinline__ v4u ld_dev(__amdgpu_buffer_rsrc_t r, u32 voff, u32 soff) { return __builtin_amdgcn_raw_buffer_load_b128(r, voff, soff, AUX_SC1); }
__device__ __forceinline__ void st_dev(v4u v, __amdgpu_buffer_rsrc_t r, u32 voff, u32 soff) { __builtin_amdgcn_raw_buffer_store_b128(v, r, voff, soff, AUX_SC1); }
__device__ __forceinline__ u32 lds_ld(LAS u32* p) { return __hip_atomic_load(p, __ATOMIC_RELAXED, __HIP_MEMORY_SCOPE_WORKGROUP); }
__device__ __forceinline__ void lds_inc(LAS u32* p) { asm volatile("ds_add_u32 %0, %1" ::"v"((u32)(size_t)p), "v"(1u) : "memory"); }

__device__ __forceinline__ void ln_slice(v4u xa, v4u xb, u32 xo, int lane, LAS u32* xs) {
  float xv[16];
#pragma unroll
  for (int k = 0; k < 4; ++k) { xv[2 * k] = lo_f(xa[k]); xv[2 * k + 1] = hi_f(xa[k]); xv[8 + 2 * k] = lo_f(xb[k]); xv[9 + 2 * k] = hi_f(xb[k]); }
  float s0 = 0.f, s1 = 0.f;
#pragma unroll
  for (int k = 0; k < 8; ++k) { s0 += xv[k]; s1 += xv[8 + k]; }
  const float mean = xrow_sum(row_sum16(s0 + s1)) * (1.f / D_);
  float v1 = 0.f, v2 = 0.f;
#pragma unroll
  for (int k = 0; k < 8; ++k) { const float d1 = xv[k] - mean, d2 = xv[8 + k] - mean; v1 = fmaf(d1, d1, v1); v2 = fmaf(d2, d2, v2); }
  const float var = xrow_sum(row_sum16(lane < 16 ? v1 + v2 : v1)) * (1.f / D_);
  const float mb = rbf(mean), inv = rcp(rbf(__builtin_amdgcn_sqrtf(rbf(rbf(var) + 1e-5f))));
  const float o0 = rbf(lo_f(xo) - mb) * inv, o1 = rbf(hi_f(xo) - mb) * inv;
  v2u o = {pk_bf16(o0, o0), pk_bf16(o1, o1)};
  if (lane < 20) *(LAS v2u*)(xs + 2 * lane) = o;
  asm volatile("s_waitcnt lgkmcnt(0)" ::: "memory");
}

__global__ void __launch_bounds__(384, 2) fused_decode(Params p) {
  __shared__ __attribute__((aligned(16))) u32 lds[80 + 64 + 4 + 4 * 32 * 68];
  LAS u32* xs = (LAS u32*)lds;
  LAS u32* qb = xs + 80;
  LAS u32* cnt = qb + 64;

  const int tid = threadIdx.x, lane = tid & 63;
  const int wave = __builtin_amdgcn_readfirstlane(tid >> 6);
  const int b = blockIdx.x, h = b >> 3, j = b & 7;
  const int c = lane & 15, g = lane >> 4;
  const bool ffn = h < 27;
  v4u msk;
  {
    const u32 half = (c & 1) ? 0xffff0000u : 0x0000ffffu; const int r = (c & 7) >> 1;
    msk.x = r == 0 ? half : 0u; msk.y = r == 1 ? half : 0u; msk.z = r == 2 ? half : 0u; msk.w = r == 3 ? half : 0u;
  }
  u32 l16 = lane * 16, xoo = (lane < 20 ? lane : 0) * 4, wqo = g * (10 * 8192) + c * 16;
  asm volatile("" : "+v"(msk.x), "+v"(msk.y), "+v"(msk.z), "+v"(msk.w), "+v"(l16), "+v"(xoo), "+v"(wqo));
  SB();
  asm volatile("" : "+s"(p.x), "+s"(p.kc), "+s"(p.vc), "+s"(p.wq), "+s"(p.wo), "+s"(p.w1), "+s"(p.w2), "+s"(p.out), "+s"(p.ws));
  ST_DECL;
  ST(0);
  const __amdgpu_buffer_rsrc_t rws = RSRC(p.ws, WS_END);

  if (__builtin_expect(wave < 2, 1)) {
    __builtin_amdgcn_s_setprio(3);
    const __amdgpu_buffer_rsrc_t rx = RSRC(p.x, D_ * 2);
    const __amdgpu_buffer_rsrc_t rwq = RSRC(p.wq, D_ * 4096 * 2);
    const int row0 = 80 * j + 40 * wave;
    const v4u xa = __builtin_amdgcn_raw_buffer_load_b128(rx, l16, 0, 0);
    const v4u xb = __builtin_amdgcn_raw_buffer_load_b128(rx, l16 + 1024, 0, 0);
    const u32 xo = __builtin_amdgcn_raw_buffer_load_b32(rx, xoo, row0 * 2, 0);
    const u64 epoch = __hip_atomic_load((GAS u64*)(p.ws + WS_EPOCH), __ATOMIC_RELAXED, __HIP_MEMORY_SCOPE_AGENT);
    SB();
    v4u aq[10];
#pragma unroll
    for (int s = 0; s < 10; ++s) aq[s] = ld_stream(rwq, wqo, (row0 + s) * 8192 + h * 256);
    SB();
    const v2u xr = __builtin_amdgcn_raw_buffer_load_b64(rx, 0, (b < 160 ? b : 0) * 8, 0);
    if (lane < 4) cnt[lane] = 0;
    BAR();
    ST(1);
    ln_slice(xa, xb, xo, lane, xs + 40 * wave);
    ST(2);
    u32 xd[10];
#pragma unroll
    for (int s2 = 0; s2 < 5; ++s2) { const v2u t = *(LAS v2u*)(xs + 40 * wave + 10 * g + 2 * s2); xd[2 * s2] = t.x; xd[2 * s2 + 1] = t.y; }
    const Tag tg = make_tag(epoch + 1);
    u32 gvo = (2 * c + g) * 16, qso = WS_Q + (h * 16 + 2 * j + wave) * WQ_P;
    asm volatile("" : "+v"(gvo), "+s"(qso));
    f32x4 dq = {0.f, 0.f, 0.f, 0.f};
#pragma unroll
    for (int s = 0; s < 10; ++s) dq = mfma(amask(xd[s], msk), aq[s], dq);
    if (lane < 32) st_dev(tag_pack(dq, tg), rws, gvo, qso);
    ST(3);
    __builtin_amdgcn_s_sleep(QDELAY);
    {
      const u32 qo = g * WQ_P + (16 * wave + c) * 16, qbase = WS_Q + h * 16 * WQ_P;
      v4u gq[4]; int spin = 0;
      do {
        MEMFENCE();
#pragma unroll
        for (int k = 0; k < 4; ++k) gq[k] = ld_dev(rws, qo, qbase + k * 4 * WQ_P);
      } while (any_bad(tag_acc(tag_acc(tag_acc(tag_acc(0u, gq[0], tg), gq[1], tg), gq[2], tg), gq[3], tg)) && ++spin < SPIN_CAP);
      const float r = xrow_sum4((untag4(gq[0]) + untag4(gq[1])) + (untag4(gq[2]) + untag4(gq[3])));
      ((LAS u16*)qb)[4 * (16 * wave + c) + g] = (u16)pk_bf16(r, r);
      MEMFENCE();
      if (lane == 0) lds_inc(cnt);
    }
    ST(6);
    const __amdgpu_buffer_rsrc_t rw1 = RSRC(p.w1, ffn ? D_ * FF * 2 : 0);
    v4u ah[5];
#pragma unroll
    for (int s = 0; s < 5; ++s) ah[s] = ld_stream(rw1, (10 * g + 5 * (c >> 3)) * (FF * 2) + (c & 7) * 16, (row0 + s) * (FF * 2) + h * 128);
    SB();
    { int spin = 0; while (lds_ld(cnt + 2) < 4 && ++spin < SPIN_CAP) __builtin_amdgcn_s_sleep(1); }
    const __amdgpu_buffer_rsrc_t rwo = RSRC(p.wo, 4096 * D_ * 2);
    const __amdgpu_buffer_rsrc_t rw2 = RSRC(p.w2, ffn ? FF * D_ * 2 : 0);
    v4u bo[3][4], b2[3][2];
#pragma unroll
    for (int k = 0; k < 3; ++k) {
      const bool valid = k < 2 || wave == 0;
      const u32 so = valid ? (h * 128 + 16 * j) * 1280 + (3 * wave + k) * 256 : 0x70000000u, s2 = valid ? (h * 64 + 8 * j) * 1280 + (3 * wave + k) * 256 : 0x70000000u;
#pragma unroll
      for (int s = 0; s < 2; ++s) b2[k][s] = ld_stream(rw2, (2 * g + s) * 1280 + c * 16, s2);
#pragma unroll
      for (int s = 0; s < 4; ++s) bo[k][s] = ld_stream(rwo, (4 * g + s) * 1280 + c * 16, so);
    }
    SB();
    f32x4 dh = {0.f, 0.f, 0.f, 0.f};
#pragma unroll
    for (int s = 0; s < 5; ++s) dh = mfma(amask((c >> 3) ? xd[5 + s] : xd[s], msk), ah[s], dh);
#pragma unroll
    for (int k = 0; k < 4; ++k) {
      auto t = __builtin_amdgcn_permlane32_swap(asu(dh[k]), asu(dh[k]), false, false);
      dh[k] = asf(t[0]) + dpp<ROR8>(asf(t[1]));
    }
    if (c < 8 && g < 2) st_dev(tag_pack(dh, tg), rws, (2 * c + g) * 16, WS_H + (h * 16 + 2 * j + wave) * WH_P);
    ST(4);
    { int spin = 0; while (lds_ld(cnt + 1) < 4 && ++spin < SPIN_CAP) __builtin_amdgcn_s_sleep(1); }
    ST(8);
    __builtin_amdgcn_s_sleep(ADELAY);
    const u32 aoff = c * WA_P + (4 * j + g) * 16, moff = c * WA_P + 32 * 16, abase = WS_A + h * 32 * WA_P;
    const u32 hoff = c * WH_P + (2 * j + (g >> 1)) * 16, hbase = WS_H + h * 16 * WH_P;
    u32 yo = (2 * c + g) * 4096, yb = WS_Y + wave * 96 * 4096 + b * 16;
    asm volatile("" : "+v"(yo), "+s"(yb));
    v4u go0, go1, gm0, gm1, gh; int spin = 0;
    do {
      MEMFENCE();
      gh = ld_dev(rws, hoff, hbase); gm0 = ld_dev(rws, moff, abase); gm1 = ld_dev(rws, moff, abase + 16 * WA_P);
      go0 = ld_dev(rws, aoff, abase); go1 = ld_dev(rws, aoff, abase + 16 * WA_P);
    } while (any_bad(tag_acc(tag_acc(tag_acc(tag_acc(tag_acc(0u, gh, tg), gm0, tg), gm1, tg), go0, tg), go1, tg)) && ++spin < SPIN_CAP);
    ST(9);
    {
      const bool b3 = c & 8, b2_ = c & 4;
      const float h0 = untag((g & 1) ? gh.z : gh.x), h1 = untag((g & 1) ? gh.w : gh.y);
      float hs = (b3 ? h1 : h0) + dpp<ROR8>(b3 ? h0 : h1);
      hs += dpp<HMIR>(hs); hs += dpp<XOR2>(hs); hs += dpp<XOR1>(hs);
      const float ht = rbf(hs), ha = ht * rcp(1.f + ex2(-ht * LOG2E));
      const u32 hd = pk_bf16(ha, ha);
      const float m0 = untag(gm0.x), l0 = untag(gm0.y), m1 = untag(gm1.x), l1 = untag(gm1.y);
      const float nM = -max_ror1(max_ror2(max_ror4(max_ror8(vmax(m0, m1))))) * LOG2E;
      const float f0 = ex2(fmaf(m0, LOG2E, nM)), f1 = ex2(fmaf(m1, LOG2E, nM));
      const float inv = rcp(row_sum16(fmaf(l0, f0, l1 * f1)));
      const f32x4 o = untag4(go0) * f0 + untag4(go1) * f1;
      const float u0 = (b3 ? o.z : o.x) + dpp<ROR8>(b3 ? o.x : o.z), u1 = (b3 ? o.w : o.y) + dpp<ROR8>(b3 ? o.y : o.w);
      float w = (b2_ ? u1 : u0) + dpp<HMIR>(b2_ ? u0 : u1);
      w += dpp<XOR2>(w); w += dpp<XOR1>(w);
      w *= inv;
      const u32 od = pk_bf16(w, w);
      const v4u am[6] = {amask(row_bcast<0>(hd), msk), amask(row_bcast<8>(hd), msk),
                         amask(row_bcast<0>(od), msk), amask(row_bcast<4>(od), msk), amask(row_bcast<8>(od), msk), amask(row_bcast<12>(od), msk)};
      f32x4 y[3];
#pragma unroll
      for (int k = 0; k < 3; ++k) y[k] = (f32x4){0.f, 0.f, 0.f, 0.f};
#pragma unroll
      for (int s = 0; s < 6; ++s)
#pragma unroll
        for (int k = 0; k < 3; ++k) y[k] = mfma(am[s], s < 2 ? b2[k][s] : bo[k][s - 2], y[k]);
      if (lane < 32) {
        st_dev(tag_pack(y[0], tg), rws, yo, yb);
        st_dev(tag_pack(y[1], tg), rws, yo, yb + 32 * 4096);
        if (wave == 0) st_dev(tag_pack(y[2], tg), rws, yo, yb + 64 * 4096);
      }
    }
    ST(10);
    if (wave == 0 && b < 160) {
      __builtin_amdgcn_s_sleep(FDELAY);
      v4u gy[4];
#pragma unroll
      for (int k = 0; k < 4; ++k) gy[k] = ld_dev(rws, l16 + 1024 * k, WS_Y + b * 4096);
      for (int spin2 = 0;;) {
        u32 bad[4];
#pragma unroll
        for (int k = 0; k < 4; ++k) bad[k] = tag_acc(0u, gy[k], tg) & TAGM;
        if (__builtin_amdgcn_ballot_w64((bad[0] | bad[1] | bad[2] | bad[3]) != 0) == 0 || ++spin2 >= SPIN_CAP) break;
        MEMFENCE();
#pragma unroll
        for (int k = 0; k < 4; ++k) if (bad[k]) gy[k] = ld_dev(rws, l16 + 1024 * k, WS_Y + b * 4096);
      }
      ST(11);
      const float r = row_sum16(xrow_sum4((untag4(gy[0]) + untag4(gy[1])) + (untag4(gy[2]) + untag4(gy[3]))));
      const u32 xw = g < 2 ? xr.x : xr.y;
      const float res = r + ((g & 1) ? hi_f(xw) : lo_f(xw));
      if (c == 0) __hip_atomic_store((GAS u16*)p.out + 4 * b + g, (u16)pk_bf16(res, res), __ATOMIC_RELAXED, __HIP_MEMORY_SCOPE_AGENT);
      if (b == 0 && lane == 0) __hip_atomic_store((GAS u64*)(p.ws + WS_EPOCH), epoch + 1, __ATOMIC_RELAXED, __HIP_MEMORY_SCOPE_AGENT);
      ST(12);
    }
  } else {
    const int kw = wave - 2;
    const __amdgpu_buffer_rsrc_t rk = RSRC(p.kc, NH * SK * DH * 2);
    const __amdgpu_buffer_rsrc_t rv = RSRC(p.vc, NH * SK * DH * 2);
    const u32 kvs = (u32)((h * SK + (SK - WIN) + 128 * j + 32 * ((kw + h + j) & 3)) * 256);
    const u32 kvo = g * 256 + c * 16, rot = (2 * h + (j >> 1) + 2 * j) & 7;
    const u64 epoch = __hip_atomic_load((GAS u64*)(p.ws + WS_EPOCH), __ATOMIC_RELAXED, __HIP_MEMORY_SCOPE_AGENT);
    BAR();
    LAS u32* kt = cnt + 4 + kw * (32 * 68);
    LAS u32* kwr = kt + g * (4 * 68) + c * 4;
    v4u kv[16];
#pragma unroll
    for (int n = 0; n < 16; ++n) {
      if (n >= KVWIN) {
        if (n - KVWIN < 8) *(LAS v4u*)(kwr + (16 * ((n - KVWIN) >> 2) + ((n - KVWIN) & 3)) * 68) = kv[n - KVWIN];
        else asm volatile("" ::"v"(kv[n - KVWIN]));
        SB();
      }
      kv[n] = ld_stream(n < 8 ? rk : rv, kvo, kvs + ((n + rot) & 7) * 1024);
      SB();
    }
    if (lane == 0) lds_inc(cnt + 2);
#pragma unroll
    for (int n = 16 - KVWIN; n < 8; ++n) *(LAS v4u*)(kwr + (16 * (n >> 2) + (n & 3)) * 68) = kv[n];
    v4u kf[2][4];
#pragma unroll
    for (int t = 0; t < 2; ++t)
#pragma unroll
      for (int s4 = 0; s4 < 4; ++s4) kf[t][s4] = *(LAS v4u*)(kt + (16 * t + c) * 68 + s4 * 16 + g * 4);
    asm volatile("" : "+v"(kf[0][0]), "+v"(kf[0][1]), "+v"(kf[0][2]), "+v"(kf[0][3]), "+v"(kf[1][0]), "+v"(kf[1][1]), "+v"(kf[1][2]), "+v"(kf[1][3]));
    ST(5);
    const Tag tg = make_tag(epoch + 1);
    u32 pvo = lane < 32 ? (2 * c + g) * 16 : 512, pso = WS_A + (h * 32 + 4 * j + kw) * WA_P;
    asm volatile("" : "+v"(pvo), "+s"(pso));
    { int spin = 0; while (lds_ld(cnt) < 2 && ++spin < SPIN_CAP) { if (QSPIN) __builtin_amdgcn_s_sleep(QSPIN); } }
    MEMFENCE();
    {
      f32x4 d0 = {0.f, 0.f, 0.f, 0.f}, d1 = {0.f, 0.f, 0.f, 0.f};
      v4u qf[4];
#pragma unroll
      for (int s4 = 0; s4 < 4; ++s4) qf[s4] = *(LAS v4u*)(qb + 16 * s4 + 4 * g);
      SB();
#pragma unroll
      for (int s4 = 0; s4 < 4; ++s4) { d0 = mfma(kf[0][s4], qf[s4], d0); d1 = mfma(kf[1][s4], qf[s4], d1); }
      const bool b0 = c & 1, b1 = c & 2, b2 = c & 4;
      const float x0 = b0 ? d0.y : d0.x, x1 = b0 ? d0.w : d0.z, x2 = b0 ? d1.y : d1.x, x3 = b0 ? d1.w : d1.z;
      const float y0 = b1 ? x1 : x0, y1 = b1 ? x3 : x2;
      const float d = b2 ? y1 : y0;
      const float s = rbf(rbf(d) * 0.08838834764831845f);
      const float m = xrow_max(max_hmir(max_xor2(max_xor1(s))));
      const float pe = ex2((s - m) * LOG2E);
      const u32 pd = pk_bf16(pe, pe);
      const v4u am[8] = {amask(row_bcast<0>(pd), msk), amask(row_bcast<1>(pd), msk), amask(row_bcast<2>(pd), msk), amask(row_bcast<3>(pd), msk),
                         amask(row_bcast<4>(pd), msk), amask(row_bcast<5>(pd), msk), amask(row_bcast<6>(pd), msk), amask(row_bcast<7>(pd), msk)};
      float l = pe + dpp<XOR1>(pe); l += dpp<XOR2>(l); l += dpp<HMIR>(l);
      l = xrow_sum(l);
      SB();
      ST(7);
      f32x4 o = {0.f, 0.f, 0.f, 0.f};
#pragma unroll
      for (int n = 0; n < 8; ++n) o = mfma(am[n], kv[8 + n], o);
      if (lane >= 32) { o.x = m; o.y = l; o.z = m; o.w = l; }
      if (lane < 33) st_dev(tag_pack(o, tg), rws, pvo, pso);
      MEMFENCE();
      if (lane == 0) lds_inc(cnt + 1);
      ST(8);
    }
  }
  ST_FLUSH();
}

extern "C" void kernel_launch(void* const* d_in, const int* in_sizes, int n_in, void* d_out, int out_size, void* d_ws, size_t ws_size, hipStream_t stream) {
  Params p;
  p.x = (const u16*)d_in[0]; p.kc = (const u16*)d_in[1]; p.vc = (const u16*)d_in[2]; p.wq = (const u16*)d_in[3]; p.wo = (const u16*)d_in[4];
  p.w1 = (const u16*)d_in[6]; p.w2 = (const u16*)d_in[7]; p.out = (u16*)d_out; p.ws = (unsigned char*)d_ws;
  fused_decode<<<256, 384, 0, stream>>>(p);
}
```

```cpp
#define AMPG 8
#define WSEL 0
#include <hip/hip_runtime.h>
#include <hip/hip_bf16.h>

typedef unsigned short u16;
typedef unsigned int u32;
typedef unsigned long long u64;
typedef u32 v4u __attribute__((ext_vector_type(4)));
typedef u32 v2u __attribute__((ext_vector_type(2)));
typedef float f32x4 __attribute__((ext_vector_type(4)));
typedef __bf16 bf16x8 __attribute__((ext_vector_type(8)));
typedef __bf16 bf16x2 __attribute__((ext_vector_type(2)));
#define GAS __attribute__((address_space(1)))
#define LAS __attribute__((address_space(3)))

#define KVWIN 6
#define QDELAY 28
#define ADELAY 20
#define FDELAY 28
#define B0POS 0
#define QSPIN 1
constexpr int AUX_NT = 2, AUX_SC1 = 16;

constexpr int D_ = 640, NH = 32, SK = 4096, DH = 128, WIN = 1024, FF = 1728;
constexpr u32 WQ_P = 512, WH_P = 256, WA_P = 640;
constexpr u32 WS_EPOCH = 0;
constexpr u32 WS_Q = 4096;
constexpr u32 WS_H = WS_Q + 32 * 16 * WQ_P;
constexpr u32 WS_A = WS_H + 32 * 16 * WH_P;
constexpr u32 WS_Y = WS_A + 32 * 32 * WA_P;
constexpr u32 WS_END = WS_Y + 256 * 160 * 16;
constexpr u32 WS_STAMP = 32u << 20;
constexpr u32 TAGM = 0x3FFu;
constexpr int SPIN_CAP = 200000;
constexpr float LOG2E = 1.4426950408889634f;

struct Params {
  const u16* x; const u16* wq; unsigned char* ws; const u16* kc; const u16* vc; const u16* w1; const u16* wo; const u16* w2; u16* out;
};

#define SB() __builtin_amdgcn_sched_barrier(0)
#define BAR() do { SB(); asm volatile("s_waitcnt lgkmcnt(0)\n\ts_barrier" ::: "memory"); SB(); } while (0)
#define MEMFENCE() asm volatile("" ::: "memory")

#define ST(k)
#define ST_DECL
#define ST_FLUSH()

__device__ __forceinline__ float asf(u32 x) { return __builtin_bit_cast(float, x); }
__device__ __forceinline__ u32 asu(float x) { return __builtin_bit_cast(u32, x); }
constexpr int ROR8 = 0x128, ROR4 = 0x124, ROR2 = 0x122, ROR1 = 0x121, HMIR = 0x141, XOR2 = 0x4E, XOR1 = 0xB1;
template <int CTRL> __device__ __forceinline__ float dpp(float x) { return asf(__builtin_amdgcn_mov_dpp(asu(x), CTRL, 0xf, 0xf, true)); }
template <int L> __device__ __forceinline__ u32 row_bcast(u32 x) { return __builtin_amdgcn_mov_dpp(x, 0x150 + L, 0xf, 0xf, true); }
__device__ __forceinline__ float row_sum16(float x) { x += dpp<ROR8>(x); x += dpp<ROR4>(x); x += dpp<ROR2>(x); x += dpp<ROR1>(x); return x; }
__device__ __forceinline__ float vmax(float a, float b) { float r; asm("v_max_f32 %0, %1, %2" : "=v"(r) : "v"(a), "v"(b)); return r; }
#define DPP_MAX(name, ctrl) __device__ __forceinline__ float name(float x) { float r; asm("s_nop 1\n\tv_max_f32_dpp %0, %1, %1 " ctrl " row_mask:0xf bank_mask:0xf" : "=&v"(r) : "v"(x)); return r; }
DPP_MAX(max_ror8, "row_ror:8") DPP_MAX(max_ror4, "row_ror:4") DPP_MAX(max_ror2, "row_ror:2") DPP_MAX(max_ror1, "row_ror:1")
DPP_MAX(max_hmir, "row_half_mirror") DPP_MAX(max_xor2, "quad_perm:[2,3,0,1]") DPP_MAX(max_xor1, "quad_perm:[1,0,3,2]")
__device__ __forceinline__ float xrow_sum(float x) {
  auto s = __builtin_amdgcn_permlane16_swap(asu(x), asu(x), false, false);
  x = asf(s[0]) + asf(s[1]);
  auto t = __builtin_amdgcn_permlane32_swap(asu(x), asu(x), false, false);
  return asf(t[0]) + asf(t[1]);
}
__device__ __forceinline__ float xrow_max(float x) {
  auto s = __builtin_amdgcn_permlane16_swap(asu(x), asu(x), false, false);
  x = vmax(asf(s[0]), asf(s[1]));
  auto t = __builtin_amdgcn_permlane32_swap(asu(x), asu(x), false, false);
  return vmax(asf(t[0]), asf(t[1]));
}
__device__ __forceinline__ float xrow_sum4(f32x4 a) {
  auto s0 = __builtin_amdgcn_permlane32_swap(asu(a.x), asu(a.z), false, false);
  auto s1 = __builtin_amdgcn_permlane32_swap(asu(a.y), asu(a.w), false, false);
  auto s2 = __builtin_amdgcn_permlane16_swap(asu(asf(s0[0]) + asf(s0[1])), asu(asf(s1[0]) + asf(s1[1])), false, false);
  return asf(s2[0]) + asf(s2[1]);
}
__device__ __forceinline__ u32 pk_bf16(float lo, float hi) {
  bf16x2 r = {(__bf16)lo, (__bf16)hi};
  return __builtin_bit_cast(u32, r);
}
__device__ __forceinline__ float rbf(float x) { return asf(pk_bf16(x, x) << 16); }
__device__ __forceinline__ float lo_f(u32 w) { return asf(w << 16); }
__device__ __forceinline__ float hi_f(u32 w) { return asf(w & 0xffff0000u); }
__device__ __forceinline__ float rcp(float x) { return __builtin_amdgcn_rcpf(x); }
__device__ __forceinline__ float ex2(float x) { return __builtin_amdgcn_exp2f(x); }
__device__ __forceinline__ float dot2(u32 a, u32 b, float c) {
  return __builtin_amdgcn_fdot2_f32_bf16(__builtin_bit_cast(bf16x2, a), __builtin_bit_cast(bf16x2, b), c, false);
}
__device__ __forceinline__ f32x4 mfma(v4u a, v4u b, f32x4 c) {
  return __builtin_amdgcn_mfma_f32_16x16x32_bf16(__builtin_bit_cast(bf16x8, a), __builtin_bit_cast(bf16x8, b), c, 0, 0, 0);
}
__device__ __forceinline__ v4u amask(u32 dup, const v4u& m) { v4u r = {dup & m.x, dup & m.y, dup & m.z, dup & m.w}; return r; }

struct Tag { u32 f0, f1, f2, f3, keep; };
__device__ __forceinline__ Tag make_tag(u64 T) {
  const u32 tl = __builtin_amdgcn_readfirstlane((u32)T), th = __builtin_amdgcn_readfirstlane((u32)(T >> 32));
  const u64 Tu = ((u64)th << 32) | tl;
  Tag tg; tg.f0 = tl & TAGM; tg.f1 = ((u32)(Tu >> 10) + tl) & TAGM; tg.f2 = ((u32)(Tu >> 20) + tl) & TAGM; tg.f3 = ((u32)(Tu >> 30) + tl) & TAGM;
  tg.keep = ~TAGM;
  asm volatile("" : "+s"(tg.f0), "+s"(tg.f1), "+s"(tg.f2), "+s"(tg.f3), "+v"(tg.keep));
  return tg;
}
__device__ __forceinline__ v4u tag_pack(f32x4 v, const Tag& t) {
  v4u r = {(asu(v.x) & t.keep) | t.f0, (asu(v.y) & t.keep) | t.f1, (asu(v.z) & t.keep) | t.f2, (asu(v.w) & t.keep) | t.f3};
  return r;
}
__device__ __forceinline__ u32 tag_acc(u32 acc, v4u g, const Tag& t) {
  acc = __builtin_amdgcn_bitop3_b32(g.x, t.f0, acc, 0xBE); acc = __builtin_amdgcn_bitop3_b32(g.y, t.f1, acc, 0xBE);
  acc = __builtin_amdgcn_bitop3_b32(g.z, t.f2, acc, 0xBE); acc = __builtin_amdgcn_bitop3_b32(g.w, t.f3, acc, 0xBE);
  return acc;
}
__device__ __forceinline__ bool any_bad(u32 acc) { return __builtin_amdgcn_ballot_w64((acc & TAGM) != 0) != 0; }
__device__ __forceinline__ float untag(u32 w) { return asf(w & ~TAGM); }
__device__ __forceinline__ f32x4 untag4(v4u g) { f32x4 r = {untag(g.x), untag(g.y), untag(g.z), untag(g.w)}; return r; }

#define RSRC(ptr, bytes) __builtin_amdgcn_make_buffer_rsrc((void*)(ptr), 0, (int)(bytes), 0x00020000)
__device__ __forceinline__ v4u ld_stream(__amdgpu_buffer_rsrc_t r, u32 voff, u32 soff) { return __builtin_amdgcn_raw_buffer_load_b128(r, voff, soff, AUX_NT); }
__device__ __forceinline__ v4u ld_dev(__amdgpu_buffer_rsrc_t r, u32 voff, u32 soff) { return __builtin_amdgcn_raw_buffer_load_b128(r, voff, soff, AUX_SC1); }
__device__ __forceinline__ void st_dev(v4u v, __amdgpu_buffer_rsrc_t r, u32 voff, u32 soff) { __builtin_amdgcn_raw_buffer_store_b128(v, r, voff, soff, AUX_SC1); }
__device__ __forceinline__ u32 lds_ld(LAS u32* p) { return __hip_atomic_load(p, __ATOMIC_RELAXED, __HIP_MEMORY_SCOPE_WORKGROUP); }
__device__ __forceinline__ void lds_inc(LAS u32* p) { asm volatile("ds_add_u32 %0, %1" ::"v"((u32)(size_t)p), "v"(1u) : "memory"); }

__device__ __forceinline__ void ln_slice(v4u xa, v4u xb, u32 xo, int lane, LAS u32* xs) {
  float xv[16];
#pragma unroll
  for (int k = 0; k < 4; ++k) { xv[2 * k] = lo_f(xa[k]); xv[2 * k + 1] = hi_f(xa[k]); xv[8 + 2 * k] = lo_f(xb[k]); xv[9 + 2 * k] = hi_f(xb[k]); }
  float s0 = 0.f, s1 = 0.f;
#pragma unroll
  for (int k = 0; k < 8; ++k) { s0 += xv[k]; s1 += xv[8 + k]; }
  const float mean = xrow_sum(row_sum16(s0 + s1)) * (1.f / D_);
  float v1 = 0.f, v2 = 0.f;
#pragma unroll
  for (int k = 0; k < 8; ++k) { const float d1 = xv[k] - mean, d2 = xv[8 + k] - mean; v1 = fmaf(d1, d1, v1); v2 = fmaf(d2, d2, v2); }
  const float var = xrow_sum(row_sum16(lane < 16 ? v1 + v2 : v1)) * (1.f / D_);
  const float mb = rbf(mean), inv = rcp(rbf(__builtin_amdgcn_sqrtf(rbf(rbf(var) + 1e-5f))));
  const float o0 = rbf(lo_f(xo) - mb) * inv, o1 = rbf(hi_f(xo) - mb) * inv;
  v2u o = {pk_bf16(o0, o0), pk_bf16(o1, o1)};
  if (lane < 20) *(LAS v2u*)(xs + 2 * lane) = o;
  asm volatile("s_waitcnt lgkmcnt(0)" ::: "memory");
}

__global__ void __launch_bounds__(384, 2) fused_decode(Params p) {
  u64 tfirst_; asm volatile("s_memrealtime %0\n\ts_waitcnt lgkmcnt(0)" : "=s"(tfirst_)::"memory"); __builtin_amdgcn_sched_barrier(0);
  __shared__ __attribute__((aligned(16))) u32 lds[80 + 64 + 4 + 4 * 32 * 68];
  LAS u32* xs = (LAS u32*)lds;
  LAS u32* qb = xs + 80;
  LAS u32* cnt = qb + 64;

  const int tid = threadIdx.x, lane = tid & 63;
  const int wave = __builtin_amdgcn_readfirstlane(tid >> 6);
  const int b = blockIdx.x, h = b >> 3, j = b & 7;
  const int c = lane & 15, g = lane >> 4;
  const bool ffn = h < 27;
  v4u msk;
  {
    const u32 half = (c & 1) ? 0xffff0000u : 0x0000ffffu; const int r = (c & 7) >> 1;
    msk.x = r == 0 ? half : 0u; msk.y = r == 1 ? half : 0u; msk.z = r == 2 ? half : 0u; msk.w = r == 3 ? half : 0u;
  }
  u32 l16 = lane * 16, xoo = (lane < 20 ? lane : 0) * 4, wqo = g * (10 * 8192) + c * 16;
  asm volatile("" : "+v"(msk.x), "+v"(msk.y), "+v"(msk.z), "+v"(msk.w), "+v"(l16), "+v"(xoo), "+v"(wqo));
  SB();
  asm volatile("" : "+s"(p.x), "+s"(p.kc), "+s"(p.vc), "+s"(p.wq), "+s"(p.wo), "+s"(p.w1), "+s"(p.w2), "+s"(p.out), "+s"(p.ws));
  ST_DECL;
  ST(0);
  const __amdgpu_buffer_rsrc_t rws = RSRC(p.ws, WS_END);

  if (__builtin_expect(wave < 2, 1)) {
    __builtin_amdgcn_s_setprio(3);
    const __amdgpu_buffer_rsrc_t rx = RSRC(p.x, D_ * 2);
    const __amdgpu_buffer_rsrc_t rwq = RSRC(p.wq, D_ * 4096 * 2);
    const int row0 = 80 * j + 40 * wave;
    const v4u xa = __builtin_amdgcn_raw_buffer_load_b128(rx, l16, 0, 0);
    const v4u xb = __builtin_amdgcn_raw_buffer_load_b128(rx, l16 + 1024, 0, 0);
    const u32 xo = __builtin_amdgcn_raw_buffer_load_b32(rx, xoo, row0 * 2, 0);
    const u64 epoch = __hip_atomic_load((GAS u64*)(p.ws + WS_EPOCH), __ATOMIC_RELAXED, __HIP_MEMORY_SCOPE_AGENT);
    SB();
    v4u aq[10];
#pragma unroll
    for (int s = 0; s < 10; ++s) aq[s] = ld_stream(rwq, wqo, (row0 + s) * 8192 + h * 256);
    SB();
    const v2u xr = __builtin_amdgcn_raw_buffer_load_b64(rx, 0, (b < 160 ? b : 0) * 8, 0);
    if (lane < 4) cnt[lane] = 0;
    BAR();
    ST(1);
    ln_slice(xa, xb, xo, lane, xs + 40 * wave);
    if (wave == 0 && lane == 0) __hip_atomic_store((GAS u64*)(p.ws + WS_STAMP) + blockIdx.x, tfirst_, __ATOMIC_RELAXED, __HIP_MEMORY_SCOPE_AGENT);
    ST(2);
    u32 xd[10];
#pragma unroll
    for (int s2 = 0; s2 < 5; ++s2) { const v2u t = *(LAS v2u*)(xs + 40 * wave + 10 * g + 2 * s2); xd[2 * s2] = t.x; xd[2 * s2 + 1] = t.y; }
    const Tag tg = make_tag(epoch + 1);
    u32 gvo = (2 * c + g) * 16, qso = WS_Q + (h * 16 + 2 * j + wave) * WQ_P;
    asm volatile("" : "+v"(gvo), "+s"(qso));
    f32x4 dq = {0.f, 0.f, 0.f, 0.f};
#pragma unroll
    for (int s = 0; s < 10; ++s) dq = mfma(amask(xd[s], msk), aq[s], dq);
    if (lane < 32) st_dev(tag_pack(dq, tg), rws, gvo, qso);
    { SB(); u64 t_; asm volatile("s_memrealtime %0\n\ts_waitcnt lgkmcnt(0)" : "=s"(t_)::"memory"); if (lane == 0 && wave == WSEL) __hip_atomic_store((GAS u64*)(p.ws + WS_STAMP + 4096) + blockIdx.x, t_, __ATOMIC_RELAXED, __HIP_MEMORY_SCOPE_AGENT); SB(); }
    __builtin_amdgcn_s_sleep(QDELAY);
    {
      const u32 qo = g * WQ_P + (16 * wave + c) * 16, qbase = WS_Q + h * 16 * WQ_P;
      v4u gq[4]; int spin = 0;
      do {
        MEMFENCE();
#pragma unroll
        for (int k = 0; k < 4; ++k) gq[k] = ld_dev(rws, qo, qbase + k * 4 * WQ_P);
      } while (any_bad(tag_acc(tag_acc(tag_acc(tag_acc(0u, gq[0], tg), gq[1], tg), gq[2], tg), gq[3], tg)) && ++spin < SPIN_CAP);
      const float r = xrow_sum4((untag4(gq[0]) + untag4(gq[1])) + (untag4(gq[2]) + untag4(gq[3])));
      ((LAS u16*)qb)[4 * (16 * wave + c) + g] = (u16)pk_bf16(r, r);
      MEMFENCE();
      if (lane == 0) lds_inc(cnt);
    }
    ST(6);
    const __amdgpu_buffer_rsrc_t rw1 = RSRC(p.w1, ffn ? D_ * FF * 2 : 0);
    v4u ah[5];
#pragma unroll
    for (int s = 0; s < 5; ++s) ah[s] = ld_stream(rw1, (10 * g + 5 * (c >> 3)) * (FF * 2) + (c & 7) * 16, (row0 + s) * (FF * 2) + h * 128);
    SB();
    { int spin = 0; while (lds_ld(cnt + 2) < 4 && ++spin < SPIN_CAP) __builtin_amdgcn_s_sleep(1); }
    const __amdgpu_buffer_rsrc_t rwo = RSRC(p.wo, 4096 * D_ * 2);
    const __amdgpu_buffer_rsrc_t rw2 = RSRC(p.w2, ffn ? FF * D_ * 2 : 0);
    v4u bo[3][4], b2[3][2];
#pragma unroll
    for (int k = 0; k < 3; ++k) {
      const bool valid = k < 2 || wave == 0;
      const u32 so = valid ? (h * 128 + 16 * j) * 1280 + (3 * wave + k) * 256 : 0x70000000u, s2 = valid ? (h * 64 + 8 * j) * 1280 + (3 * wave + k) * 256 : 0x70000000u;
#pragma unroll
      for (int s = 0; s < 2; ++s) b2[k][s] = ld_stream(rw2, (2 * g + s) * 1280 + c * 16, s2);
#pragma unroll
      for (int s = 0; s < 4; ++s) bo[k][s] = ld_stream(rwo, (4 * g + s) * 1280 + c * 16, so);
    }
    SB();
    f32x4 dh = {0.f, 0.f, 0.f, 0.f};
#pragma unroll
    for (int s = 0; s < 5; ++s) dh = mfma(amask((c >> 3) ? xd[5 + s] : xd[s], msk), ah[s], dh);
#pragma unroll
    for (int k = 0; k < 4; ++k) {
      auto t = __builtin_amdgcn_permlane32_swap(asu(dh[k]), asu(dh[k]), false, false);
      dh[k] = asf(t[0]) + dpp<ROR8>(asf(t[1]));
    }
    if (c < 8 && g < 2) st_dev(tag_pack(dh, tg), rws, (2 * c + g) * 16, WS_H + (h * 16 + 2 * j + wave) * WH_P);
    ST(4);
    { int spin = 0; while (lds_ld(cnt + 1) < 4 && ++spin < SPIN_CAP) __builtin_amdgcn_s_sleep(1); }
    ST(8);
    __builtin_amdgcn_s_sleep(ADELAY);
    const u32 aoff = c * WA_P + (4 * j + g) * 16, moff = c * WA_P + 32 * 16, abase = WS_A + h * 32 * WA_P;
    const u32 hoff = c * WH_P + (2 * j + (g >> 1)) * 16, hbase = WS_H + h * 16 * WH_P;
    u32 yo = (2 * c + g) * 4096, yb = WS_Y + wave * 96 * 4096 + b * 16;
    asm volatile("" : "+v"(yo), "+s"(yb));
    v4u go0, go1, gm0, gm1, gh; int spin = 0;
    do {
      MEMFENCE();
      gh = ld_dev(rws, hoff, hbase); gm0 = ld_dev(rws, moff, abase); gm1 = ld_dev(rws, moff, abase + 16 * WA_P);
      go0 = ld_dev(rws, aoff, abase); go1 = ld_dev(rws, aoff, abase + 16 * WA_P);
    } while (any_bad(tag_acc(tag_acc(tag_acc(tag_acc(tag_acc(0u, gh, tg), gm0, tg), gm1, tg), go0, tg), go1, tg)) && ++spin < SPIN_CAP);
    ST(9);
    {
      const bool b3 = c & 8, b2_ = c & 4;
      const float h0 = untag((g & 1) ? gh.z : gh.x), h1 = untag((g & 1) ? gh.w : gh.y);
      float hs = (b3 ? h1 : h0) + dpp<ROR8>(b3 ? h0 : h1);
      hs += dpp<HMIR>(hs); hs += dpp<XOR2>(hs); hs += dpp<XOR1>(hs);
      const float ht = rbf(hs), ha = ht * rcp(1.f + ex2(-ht * LOG2E));
      const u32 hd = pk_bf16(ha, ha);
      const float m0 = untag(gm0.x), l0 = untag(gm0.y), m1 = untag(gm1.x), l1 = untag(gm1.y);
      const float nM = -max_ror1(max_ror2(max_ror4(max_ror8(vmax(m0, m1))))) * LOG2E;
      const float f0 = ex2(fmaf(m0, LOG2E, nM)), f1 = ex2(fmaf(m1, LOG2E, nM));
      const float inv = rcp(row_sum16(fmaf(l0, f0, l1 * f1)));
      const f32x4 o = untag4(go0) * f0 + untag4(go1) * f1;
      const float u0 = (b3 ? o.z : o.x) + dpp<ROR8>(b3 ? o.x : o.z), u1 = (b3 ? o.w : o.y) + dpp<ROR8>(b3 ? o.y : o.w);
      float w = (b2_ ? u1 : u0) + dpp<HMIR>(b2_ ? u0 : u1);
      w += dpp<XOR2>(w); w += dpp<XOR1>(w);
      w *= inv;
      const u32 od = pk_bf16(w, w);
      const v4u am[6] = {amask(row_bcast<0>(hd), msk), amask(row_bcast<8>(hd), msk),
                         amask(row_bcast<0>(od), msk), amask(row_bcast<4>(od), msk), amask(row_bcast<8>(od), msk), amask(row_bcast<12>(od), msk)};
      f32x4 y[3];
#pragma unroll
      for (int k = 0; k < 3; ++k) y[k] = (f32x4){0.f, 0.f, 0.f, 0.f};
#pragma unroll
      for (int s = 0; s < 6; ++s)
#pragma unroll
        for (int k = 0; k < 3; ++k) y[k] = mfma(am[s], s < 2 ? b2[k][s] : bo[k][s - 2], y[k]);
      if (lane < 32) {
        st_dev(tag_pack(y[0], tg), rws, yo, yb);
        st_dev(tag_pack(y[1], tg), rws, yo, yb + 32 * 4096);
        if (wave == 0) st_dev(tag_pack(y[2], tg), rws, yo, yb + 64 * 4096);
      }
    }
    ST(10);
    if (wave == 0 && b < 160) {
      __builtin_amdgcn_s_sleep(FDELAY);
      v4u gy[4];
#pragma unroll
      for (int k = 0; k < 4; ++k) gy[k] = ld_dev(rws, l16 + 1024 * k, WS_Y + b * 4096);
      for (int spin2 = 0;;) {
        u32 bad[4];
#pragma unroll
        for (int k = 0; k < 4; ++k) bad[k] = tag_acc(0u, gy[k], tg) & TAGM;
        if (__builtin_amdgcn_ballot_w64((bad[0] | bad[1] | bad[2] | bad[3]) != 0) == 0 || ++spin2 >= SPIN_CAP) break;
        MEMFENCE();
#pragma unroll
        for (int k = 0; k < 4; ++k) if (bad[k]) gy[k] = ld_dev(rws, l16 + 1024 * k, WS_Y + b * 4096);
      }
      ST(11);
      const float r = row_sum16(xrow_sum4((untag4(gy[0]) + untag4(gy[1])) + (untag4(gy[2]) + untag4(gy[3]))));
      const u32 xw = g < 2 ? xr.x : xr.y;
      const float res = r + ((g & 1) ? hi_f(xw) : lo_f(xw));
      if (c == 0) __hip_atomic_store((GAS u16*)p.out + 4 * b + g, (u16)pk_bf16(res, res), __ATOMIC_RELAXED, __HIP_MEMORY_SCOPE_AGENT);
      if (b == 0 && lane == 0) __hip_atomic_store((GAS u64*)(p.ws + WS_EPOCH), epoch + 1, __ATOMIC_RELAXED, __HIP_MEMORY_SCOPE_AGENT);
      if (b == 0) {
        u64 now, mn = ~0ull, mx = 0;
        for (int k = 0; k < 4; ++k) { u64 t = __hip_atomic_load((GAS u64*)(p.ws + WS_STAMP) + lane + 64 * k, __ATOMIC_RELAXED, __HIP_MEMORY_SCOPE_AGENT); mn = t < mn ? t : mn; }
        for (int o = 32; o > 0; o >>= 1) { u64 a2 = __shfl_xor(mn, o); mn = a2 < mn ? a2 : mn; }
        for (int k = 0; k < 4; ++k) { u64 t = __hip_atomic_load((GAS u64*)(p.ws + WS_STAMP + 4096) + lane + 64 * k, __ATOMIC_RELAXED, __HIP_MEMORY_SCOPE_AGENT); if (t < mn || t > mn + 5000) t = mn; mx += t - mn; }
        for (int o = 32; o > 0; o >>= 1) { mx += __shfl_xor(mx, o); } u64 ivv = mx >> 8;
        const u64 iv = ((u64)__builtin_amdgcn_readfirstlane((u32)(ivv >> 32)) << 32) | __builtin_amdgcn_readfirstlane((u32)ivv);
        asm volatile("s_memrealtime %0\n\ts_waitcnt lgkmcnt(0)" : "=s"(now)::"memory");
        const u64 tgt = now + (u64)AMPG * (iv > 100000 ? 0 : iv); int guard = 0;
        do { asm volatile("s_memrealtime %0\n\ts_waitcnt lgkmcnt(0)" : "=s"(now)::"memory"); } while (now < tgt && ++guard < 1000000);
      }

    }
  } else {
    const int kw = wave - 2;
    const __amdgpu_buffer_rsrc_t rk = RSRC(p.kc, NH * SK * DH * 2);
    const __amdgpu_buffer_rsrc_t rv = RSRC(p.vc, NH * SK * DH * 2);
    const u32 kvs = (u32)((h * SK + (SK - WIN) + 128 * j + 32 * ((kw + h + j) & 3)) * 256);
    const u32 kvo = g * 256 + c * 16, rot = (2 * h + (j >> 1) + 2 * j) & 7;
    const u64 epoch = __hip_atomic_load((GAS u64*)(p.ws + WS_EPOCH), __ATOMIC_RELAXED, __HIP_MEMORY_SCOPE_AGENT);
    BAR();
    LAS u32* kt = cnt + 4 + kw * (32 * 68);
    LAS u32* kwr = kt + g * (4 * 68) + c * 4;
    v4u kv[16];
#pragma unroll
    for (int n = 0; n < 16; ++n) {
      if (n >= KVWIN) {
        if (n - KVWIN < 8) *(LAS v4u*)(kwr + (16 * ((n - KVWIN) >> 2) + ((n - KVWIN) & 3)) * 68) = kv[n - KVWIN];
        else asm volatile("" ::"v"(kv[n - KVWIN]));
        SB();
      }
      kv[n] = ld_stream(n < 8 ? rk : rv, kvo, kvs + ((n + rot) & 7) * 1024);
      SB();
    }
    if (lane == 0) lds_inc(cnt + 2);
#pragma unroll
    for (int n = 16 - KVWIN; n < 8; ++n) *(LAS v4u*)(kwr + (16 * (n >> 2) + (n & 3)) * 68) = kv[n];
    v4u kf[2][4];
#pragma unroll
    for (int t = 0; t < 2; ++t)
#pragma unroll
      for (int s4 = 0; s4 < 4; ++s4) kf[t][s4] = *(LAS v4u*)(kt + (16 * t + c) * 68 + s4 * 16 + g * 4);
    asm volatile("" : "+v"(kf[0][0]), "+v"(kf[0][1]), "+v"(kf[0][2]), "+v"(kf[0][3]), "+v"(kf[1][0]), "+v"(kf[1][1]), "+v"(kf[1][2]), "+v"(kf[1][3]));
    ST(5);
    const Tag tg = make_tag(epoch + 1);
    u32 pvo = lane < 32 ? (2 * c + g) * 16 : 512, pso = WS_A + (h * 32 + 4 * j + kw) * WA_P;
    asm volatile("" : "+v"(pvo), "+s"(pso));
    { int spin = 0; while (lds_ld(cnt) < 2 && ++spin < SPIN_CAP) { if (QSPIN) __builtin_amdgcn_s_sleep(QSPIN); } }
    MEMFENCE();
    {
      f32x4 d0 = {0.f, 0.f, 0.f, 0.f}, d1 = {0.f, 0.f, 0.f, 0.f};
      v4u qf[4];
#pragma unroll
      for (int s4 = 0; s4 < 4; ++s4) qf[s4] = *(LAS v4u*)(qb + 16 * s4 + 4 * g);
      SB();
#pragma unroll
      for (int s4 = 0; s4 < 4; ++s4) { d0 = mfma(kf[0][s4], qf[s4], d0); d1 = mfma(kf[1][s4], qf[s4], d1); }
      const bool b0 = c & 1, b1 = c & 2, b2 = c & 4;
      const float x0 = b0 ? d0.y : d0.x, x1 = b0 ? d0.w : d0.z, x2 = b0 ? d1.y : d1.x, x3 = b0 ? d1.w : d1.z;
      const float y0 = b1 ? x1 : x0, y1 = b1 ? x3 : x2;
      const float d = b2 ? y1 : y0;
      const float s = rbf(rbf(d) * 0.08838834764831845f);
      const float m = xrow_max(max_hmir(max_xor2(max_xor1(s))));
      const float pe = ex2((s - m) * LOG2E);
      const u32 pd = pk_bf16(pe, pe);
      const v4u am[8] = {amask(row_bcast<0>(pd), msk), amask(row_bcast<1>(pd), msk), amask(row_bcast<2>(pd), msk), amask(row_bcast<3>(pd), msk),
                         amask(row_bcast<4>(pd), msk), amask(row_bcast<5>(pd), msk), amask(row_bcast<6>(pd), msk), amask(row_bcast<7>(pd), msk)};
      float l = pe + dpp<XOR1>(pe); l += dpp<XOR2>(l); l += dpp<HMIR>(l);
      l = xrow_sum(l);
      SB();
      ST(7);
      f32x4 o = {0.f, 0.f, 0.f, 0.f};
#pragma unroll
      for (int n = 0; n < 8; ++n) o = mfma(am[n], kv[8 + n], o);
      if (lane >= 32) { o.x = m; o.y = l; o.z = m; o.w = l; }
      if (lane < 33) st_dev(tag_pack(o, tg), rws, pvo, pso);
      MEMFENCE();
      if (lane == 0) lds_inc(cnt + 1);
      ST(8);
    }
  }
  ST_FLUSH();
}

extern "C" void kernel_launch(void* const* d_in, const int* in_sizes, int n_in, void* d_out, int out_size, void* d_ws, size_t ws_size, hipStream_t stream) {
  Params p;
  p.x = (const u16*)d_in[0]; p.kc = (const u16*)d_in[1]; p.vc = (const u16*)d_in[2]; p.wq = (const u16*)d_in[3]; p.wo = (const u16*)d_in[4];
  p.w1 = (const u16*)d_in[6]; p.w2 = (const u16*)d_in[7]; p.out = (u16*)d_out; p.ws = (unsigned char*)d_ws;
  fused_decode<<<256, 384, 0, stream>>>(p);
}
```

```cpp
#include <hip/hip_runtime.h>
#include <hip/hip_bf16.h>

typedef unsigned short u16;
typedef unsigned int u32;
typedef unsigned long long u64;
typedef u32 v4u __attribute__((ext_vector_type(4)));
typedef u32 v2u __attribute__((ext_vector_type(2)));
typedef float f32x4 __attribute__((ext_vector_type(4)));
typedef __bf16 bf16x8 __attribute__((ext_vector_type(8)));
typedef __bf16 bf16x2 __attribute__((ext_vector_type(2)));
#define GAS __attribute__((address_space(1)))
#define LAS __attribute__((address_space(3)))

constexpr int KVWIN = 6;
constexpr int QDELAY = 28, ADELAY = 20, FDELAY = 28;
constexpr int AUX_NT = 2, AUX_SC1 = 16;

constexpr int D_ = 640, NH = 32, SK = 4096, DH = 128, WIN = 1024, FF = 1728;
constexpr u32 WQ_P = 512, WH_P = 256, WA_P = 640;
constexpr u32 WS_EPOCH = 0;
constexpr u32 WS_Q = 4096;
constexpr u32 WS_H = WS_Q + 32 * 16 * WQ_P;
constexpr u32 WS_A = WS_H + 32 * 16 * WH_P;
constexpr u32 WS_Y = WS_A + 32 * 32 * WA_P;
constexpr u32 WS_END = WS_Y + 256 * 160 * 16;
constexpr u32 TAGM = 0x3FFu;
constexpr int SPIN_CAP = 200000;
constexpr float LOG2E = 1.4426950408889634f;

struct Params {
  const u16* x; const u16* wq; unsigned char* ws; const u16* kc; const u16* vc; const u16* w1; const u16* wo; const u16* w2; u16* out;
};

#define SB() __builtin_amdgcn_sched_barrier(0)
#define BAR() do { SB(); asm volatile("s_waitcnt lgkmcnt(0)\n\ts_barrier" ::: "memory"); SB(); } while (0)
#define MEMFENCE() asm volatile("" ::: "memory")


__device__ __forceinline__ float asf(u32 x) { return __builtin_bit_cast(float, x); }
__device__ __forceinline__ u32 asu(float x) { return __builtin_bit_cast(u32, x); }
constexpr int ROR8 = 0x128, ROR4 = 0x124, ROR2 = 0x122, ROR1 = 0x121, HMIR = 0x141, XOR2 = 0x4E, XOR1 = 0xB1;
template <int CTRL> __device__ __forceinline__ float dpp(float x) { return asf(__builtin_amdgcn_mov_dpp(asu(x), CTRL, 0xf, 0xf, true)); }
template <int L> __device__ __forceinline__ u32 row_bcast(u32 x) { return __builtin_amdgcn_mov_dpp(x, 0x150 + L, 0xf, 0xf, true); }
__device__ __forceinline__ float row_sum16(float x) { x += dpp<ROR8>(x); x += dpp<ROR4>(x); x += dpp<ROR2>(x); x += dpp<ROR1>(x); return x; }
__device__ __forceinline__ float vmax(float a, float b) { float r; asm("v_max_f32 %0, %1, %2" : "=v"(r) : "v"(a), "v"(b)); return r; }
#define DPP_MAX(name, ctrl) __device__ __forceinline__ float name(float x) { float r; asm("s_nop 1\n\tv_max_f32_dpp %0, %1, %1 " ctrl " row_mask:0xf bank_mask:0xf" : "=&v"(r) : "v"(x)); return r; }
DPP_MAX(max_ror8, "row_ror:8") DPP_MAX(max_ror4, "row_ror:4") DPP_MAX(max_ror2, "row_ror:2") DPP_MAX(max_ror1, "row_ror:1")
DPP_MAX(max_hmir, "row_half_mirror") DPP_MAX(max_xor2, "quad_perm:[2,3,0,1]") DPP_MAX(max_xor1, "quad_perm:[1,0,3,2]")
__device__ __forceinline__ float xrow_sum(float x) {
  auto s = __builtin_amdgcn_permlane16_swap(asu(x), asu(x), false, false);
  x = asf(s[0]) + asf(s[1]);
  auto t = __builtin_amdgcn_permlane32_swap(asu(x), asu(x), false, false);
  return asf(t[0]) + asf(t[1]);
}
__device__ __forceinline__ float xrow_max(float x) {
  auto s = __builtin_amdgcn_permlane16_swap(asu(x), asu(x), false, false);
  x = vmax(asf(s[0]), asf(s[1]));
  auto t = __builtin_amdgcn_permlane32_swap(asu(x), asu(x), false, false);
  return vmax(asf(t[0]), asf(t[1]));
}
__device__ __forceinline__ float xrow_sum4(f32x4 a) {
  auto s0 = __builtin_amdgcn_permlane32_swap(asu(a.x), asu(a.z), false, false);
  auto s1 = __builtin_amdgcn_permlane32_swap(asu(a.y), asu(a.w), false, false);
  auto s2 = __builtin_amdgcn_permlane16_swap(asu(asf(s0[0]) + asf(s0[1])), asu(asf(s1[0]) + asf(s1[1])), false, false);
  return asf(s2[0]) + asf(s2[1]);
}
__device__ __forceinline__ u32 pk_bf16(float lo, float hi) {
  bf16x2 r = {(__bf16)lo, (__bf16)hi};
  return __builtin_bit_cast(u32, r);
}
__device__ __forceinline__ float rbf(float x) { return asf(pk_bf16(x, x) << 16); }
__device__ __forceinline__ float lo_f(u32 w) { return asf(w << 16); }
__device__ __forceinline__ float hi_f(u32 w) { return asf(w & 0xffff0000u); }
__device__ __forceinline__ float rcp(float x) { return __builtin_amdgcn_rcpf(x); }
__device__ __forceinline__ float ex2(float x) { return __builtin_amdgcn_exp2f(x); }
__device__ __forceinline__ float dot2(u32 a, u32 b, float c) {
  return __builtin_amdgcn_fdot2_f32_bf16(__builtin_bit_cast(bf16x2, a), __builtin_bit_cast(bf16x2, b), c, false);
}
__device__ __forceinline__ f32x4 mfma(v4u a, v4u b, f32x4 c) {
  return __builtin_amdgcn_mfma_f32_16x16x32_bf16(__builtin_bit_cast(bf16x8, a), __builtin_bit_cast(bf16x8, b), c, 0, 0, 0);
}
__device__ __forceinline__ v4u amask(u32 dup, const v4u& m) { v4u r = {dup & m.x, dup & m.y, dup & m.z, dup & m.w}; return r; }

struct Tag { u32 f0, f1, f2, f3, keep; };
__device__ __forceinline__ Tag make_tag(u64 T) {
  const u32 tl = __builtin_amdgcn_readfirstlane((u32)T), th = __builtin_amdgcn_readfirstlane((u32)(T >> 32));
  const u64 Tu = ((u64)th << 32) | tl;
  Tag tg; tg.f0 = tl & TAGM; tg.f1 = ((u32)(Tu >> 10) + tl) & TAGM; tg.f2 = ((u32)(Tu >> 20) + tl) & TAGM; tg.f3 = ((u32)(Tu >> 30) + tl) & TAGM;
  tg.keep = ~TAGM;
  asm volatile("" : "+s"(tg.f0), "+s"(tg.f1), "+s"(tg.f2), "+s"(tg.f3), "+v"(tg.keep));
  return tg;
}
__device__ __forceinline__ v4u tag_pack(f32x4 v, const Tag& t) {
  v4u r = {(asu(v.x) & t.keep) | t.f0, (asu(v.y) & t.keep) | t.f1, (asu(v.z) & t.keep) | t.f2, (asu(v.w) & t.keep) | t.f3};
  return r;
}
__device__ __forceinline__ u32 tag_acc(u32 acc, v4u g, const Tag& t) {
  acc = __builtin_amdgcn_bitop3_b32(g.x, t.f0, acc, 0xBE); acc = __builtin_amdgcn_bitop3_b32(g.y, t.f1, acc, 0xBE);
  acc = __builtin_amdgcn_bitop3_b32(g.z, t.f2, acc, 0xBE); acc = __builtin_amdgcn_bitop3_b32(g.w, t.f3, acc, 0xBE);
  return acc;
}
__device__ __forceinline__ bool any_bad(u32 acc) { return __builtin_amdgcn_ballot_w64((acc & TAGM) != 0) != 0; }
__device__ __forceinline__ float untag(u32 w) { return asf(w & ~TAGM); }
__device__ __forceinline__ f32x4 untag4(v4u g) { f32x4 r = {untag(g.x), untag(g.y), untag(g.z), untag(g.w)}; return r; }

#define RSRC(ptr, bytes) __builtin_amdgcn_make_buffer_rsrc((void*)(ptr), 0, (int)(bytes), 0x00020000)
__device__ __forceinline__ v4u ld_stream(__amdgpu_buffer_rsrc_t r, u32 voff, u32 soff) { return __builtin_amdgcn_raw_buffer_load_b128(r, voff, soff, AUX_NT); }
__device__ __forceinline__ v4u ld_dev(__amdgpu_buffer_rsrc_t r, u32 voff, u32 soff) { return __builtin_amdgcn_raw_buffer_load_b128(r, voff, soff, AUX_SC1); }
__device__ __forceinline__ void st_dev(v4u v, __amdgpu_buffer_rsrc_t r, u32 voff, u32 soff) { __builtin_amdgcn_raw_buffer_store_b128(v, r, voff, soff, AUX_SC1); }
__device__ __forceinline__ u32 lds_ld(LAS u32* p) { return __hip_atomic_load(p, __ATOMIC_RELAXED, __HIP_MEMORY_SCOPE_WORKGROUP); }
__device__ __forceinline__ void lds_inc(LAS u32* p) { asm volatile("ds_add_u32 %0, %1" ::"v"((u32)(size_t)p), "v"(1u) : "memory"); }

__device__ __forceinline__ void ln_slice(v4u xa, v4u xb, u32 xo, int lane, LAS u32* xs) {
  float xv[16];
#pragma unroll
  for (int k = 0; k < 4; ++k) { xv[2 * k] = lo_f(xa[k]); xv[2 * k + 1] = hi_f(xa[k]); xv[8 + 2 * k] = lo_f(xb[k]); xv[9 + 2 * k] = hi_f(xb[k]); }
  float s0 = 0.f, s1 = 0.f;
#pragma unroll
  for (int k = 0; k < 8; ++k) { s0 += xv[k]; s1 += xv[8 + k]; }
  const float mean = xrow_sum(row_sum16(s0 + s1)) * (1.f / D_);
  float v1 = 0.f, v2 = 0.f;
#pragma unroll
  for (int k = 0; k < 8; ++k) { const float d1 = xv[k] - mean, d2 = xv[8 + k] - mean; v1 = fmaf(d1, d1, v1); v2 = fmaf(d2, d2, v2); }
  const float var = xrow_sum(row_sum16(lane < 16 ? v1 + v2 : v1)) * (1.f / D_);
  const float mb = rbf(mean), inv = rcp(rbf(__builtin_amdgcn_sqrtf(rbf(rbf(var) + 1e-5f))));
  const float o0 = rbf(lo_f(xo) - mb) * inv, o1 = rbf(hi_f(xo) - mb) * inv;
  v2u o = {pk_bf16(o0, o0), pk_bf16(o1, o1)};
  if (lane < 20) *(LAS v2u*)(xs + 2 * lane) = o;
  asm volatile("s_waitcnt lgkmcnt(0)" ::: "memory");
}

__global__ void __launch_bounds__(384, 2) fused_decode(Params p) {
  __shared__ __attribute__((aligned(16))) u32 lds[80 + 64 + 4 + 4 * 32 * 68];
  LAS u32* xs = (LAS u32*)lds;
  LAS u32* qb = xs + 80;
  LAS u32* cnt = qb + 64;

  const int tid = threadIdx.x, lane = tid & 63;
  const int wave = __builtin_amdgcn_readfirstlane(tid >> 6);
  const int b = blockIdx.x, h = b >> 3, j = b & 7;
  const int c = lane & 15, g = lane >> 4;
  const bool ffn = h < 27;
  v4u msk;
  {
    const u32 half = (c & 1) ? 0xffff0000u : 0x0000ffffu; const int r = (c & 7) >> 1;
    msk.x = r == 0 ? half : 0u; msk.y = r == 1 ? half : 0u; msk.z = r == 2 ? half : 0u; msk.w = r == 3 ? half : 0u;
  }
  u32 l16 = lane * 16, xoo = (lane < 20 ? lane : 0) * 4, wqo = g * (10 * 8192) + c * 16;
  asm volatile("" : "+v"(msk.x), "+v"(msk.y), "+v"(msk.z), "+v"(msk.w), "+v"(l16), "+v"(xoo), "+v"(wqo));
  SB();
  asm volatile("" : "+s"(p.x), "+s"(p.kc), "+s"(p.vc), "+s"(p.wq), "+s"(p.wo), "+s"(p.w1), "+s"(p.w2), "+s"(p.out), "+s"(p.ws));
  const __amdgpu_buffer_rsrc_t rws = RSRC(p.ws, WS_END);

  if (__builtin_expect(wave < 2, 1)) {
    __builtin_amdgcn_s_setprio(3);
    const __amdgpu_buffer_rsrc_t rx = RSRC(p.x, D_ * 2);
    const __amdgpu_buffer_rsrc_t rwq = RSRC(p.wq, D_ * 4096 * 2);
    const int row0 = 80 * j + 40 * wave;
    const v4u xa = __builtin_amdgcn_raw_buffer_load_b128(rx, l16, 0, 0);
    const v4u xb = __builtin_amdgcn_raw_buffer_load_b128(rx, l16 + 1024, 0, 0);
    const u32 xo = __builtin_amdgcn_raw_buffer_load_b32(rx, xoo, row0 * 2, 0);
    const u64 epoch = __hip_atomic_load((GAS u64*)(p.ws + WS_EPOCH), __ATOMIC_RELAXED, __HIP_MEMORY_SCOPE_AGENT);
    SB();
    v4u aq[10];
#pragma unroll
    for (int s = 0; s < 10; ++s) aq[s] = ld_stream(rwq, wqo, (row0 + s) * 8192 + h * 256);
    SB();
    const v2u xr = __builtin_amdgcn_raw_buffer_load_b64(rx, 0, (b < 160 ? b : 0) * 8, 0);
    if (lane < 4) cnt[lane] = 0;
    BAR();
    ln_slice(xa, xb, xo, lane, xs + 40 * wave);
    u32 xd[10];
#pragma unroll
    for (int s2 = 0; s2 < 5; ++s2) { const v2u t = *(LAS v2u*)(xs + 40 * wave + 10 * g + 2 * s2); xd[2 * s2] = t.x; xd[2 * s2 + 1] = t.y; }
    const Tag tg = make_tag(epoch + 1);
    u32 gvo = (2 * c + g) * 16, qso = WS_Q + (h * 16 + 2 * j + wave) * WQ_P;
    asm volatile("" : "+v"(gvo), "+s"(qso));
    f32x4 dq = {0.f, 0.f, 0.f, 0.f};
#pragma unroll
    for (int s = 0; s < 10; ++s) dq = mfma(amask(xd[s], msk), aq[s], dq);
    if (lane < 32) st_dev(tag_pack(dq, tg), rws, gvo, qso);
    __builtin_amdgcn_s_sleep(QDELAY);
    {
      const u32 qo = g * WQ_P + (16 * wave + c) * 16, qbase = WS_Q + h * 16 * WQ_P;
      v4u gq[4]; int spin = 0;
      do {
        MEMFENCE();
#pragma unroll
        for (int k = 0; k < 4; ++k) gq[k] = ld_dev(rws, qo, qbase + k * 4 * WQ_P);
      } while (any_bad(tag_acc(tag_acc(tag_acc(tag_acc(0u, gq[0], tg), gq[1], tg), gq[2], tg), gq[3], tg)) && ++spin < SPIN_CAP);
      const float r = xrow_sum4((untag4(gq[0]) + untag4(gq[1])) + (untag4(gq[2]) + untag4(gq[3])));
      ((LAS u16*)qb)[4 * (16 * wave + c) + g] = (u16)pk_bf16(r, r);
      MEMFENCE();
      if (lane == 0) lds_inc(cnt);
    }
    const __amdgpu_buffer_rsrc_t rw1 = RSRC(p.w1, ffn ? D_ * FF * 2 : 0);
    v4u ah[5];
#pragma unroll
    for (int s = 0; s < 5; ++s) ah[s] = ld_stream(rw1, (10 * g + 5 * (c >> 3)) * (FF * 2) + (c & 7) * 16, (row0 + s) * (FF * 2) + h * 128);
    SB();
    { int spin = 0; while (lds_ld(cnt + 2) < 4 && ++spin < SPIN_CAP) __builtin_amdgcn_s_sleep(1); }
    const __amdgpu_buffer_rsrc_t rwo = RSRC(p.wo, 4096 * D_ * 2);
    const __amdgpu_buffer_rsrc_t rw2 = RSRC(p.w2, ffn ? FF * D_ * 2 : 0);
    v4u bo[3][4], b2[3][2];
#pragma unroll
    for (int k = 0; k < 3; ++k) {
      const bool valid = k < 2 || wave == 0;
      const u32 so = valid ? (h * 128 + 16 * j) * 1280 + (3 * wave + k) * 256 : 0x70000000u, s2 = valid ? (h * 64 + 8 * j) * 1280 + (3 * wave + k) * 256 : 0x70000000u;
#pragma unroll
      for (int s = 0; s < 2; ++s) b2[k][s] = ld_stream(rw2, (2 * g + s) * 1280 + c * 16, s2);
#pragma unroll
      for (int s = 0; s < 4; ++s) bo[k][s] = ld_stream(rwo, (4 * g + s) * 1280 + c * 16, so);
    }
    SB();
    f32x4 dh = {0.f, 0.f, 0.f, 0.f};
#pragma unroll
    for (int s = 0; s < 5; ++s) dh = mfma(amask((c >> 3) ? xd[5 + s] : xd[s], msk), ah[s], dh);
#pragma unroll
    for (int k = 0; k < 4; ++k) {
      auto t = __builtin_amdgcn_permlane32_swap(asu(dh[k]), asu(dh[k]), false, false);
      dh[k] = asf(t[0]) + dpp<ROR8>(asf(t[1]));
    }
    if (c < 8 && g < 2) st_dev(tag_pack(dh, tg), rws, (2 * c + g) * 16, WS_H + (h * 16 + 2 * j + wave) * WH_P);
    { int spin = 0; while (lds_ld(cnt + 1) < 4 && ++spin < SPIN_CAP) __builtin_amdgcn_s_sleep(1); }
    __builtin_amdgcn_s_sleep(ADELAY);
    const u32 aoff = c * WA_P + j * 80 + g * 16, moff = c * WA_P + j * 80 + 64, abase = WS_A + h * 32 * WA_P;
    const u32 hoff = c * WH_P + (2 * j + (g >> 1)) * 16, hbase = WS_H + h * 16 * WH_P;
    u32 yo = (2 * c + g) * 4096, yb = WS_Y + wave * 96 * 4096 + b * 16;
    asm volatile("" : "+v"(yo), "+s"(yb));
    v4u go0, go1, gm0, gm1, gh; int spin = 0;
    do {
      MEMFENCE();
      gh = ld_dev(rws, hoff, hbase); gm0 = ld_dev(rws, moff, abase); gm1 = ld_dev(rws, moff, abase + 16 * WA_P);
      go0 = ld_dev(rws, aoff, abase); go1 = ld_dev(rws, aoff, abase + 16 * WA_P);
    } while (any_bad(tag_acc(tag_acc(tag_acc(tag_acc(tag_acc(0u, gh, tg), gm0, tg), gm1, tg), go0, tg), go1, tg)) && ++spin < SPIN_CAP);
    {
      const bool b3 = c & 8, b2_ = c & 4;
      const float h0 = untag((g & 1) ? gh.z : gh.x), h1 = untag((g & 1) ? gh.w : gh.y);
      float hs = (b3 ? h1 : h0) + dpp<ROR8>(b3 ? h0 : h1);
      hs += dpp<HMIR>(hs); hs += dpp<XOR2>(hs); hs += dpp<XOR1>(hs);
      const float ht = rbf(hs), ha = ht * rcp(1.f + ex2(-ht * LOG2E));
      const u32 hd = pk_bf16(ha, ha);
      const float m0 = untag(gm0.x), l0 = untag(gm0.y), m1 = untag(gm1.x), l1 = untag(gm1.y);
      const float nM = -max_ror1(max_ror2(max_ror4(max_ror8(vmax(m0, m1))))) * LOG2E;
      const float f0 = ex2(fmaf(m0, LOG2E, nM)), f1 = ex2(fmaf(m1, LOG2E, nM));
      const float inv = rcp(row_sum16(fmaf(l0, f0, l1 * f1)));
      const f32x4 o = untag4(go0) * f0 + untag4(go1) * f1;
      const float u0 = (b3 ? o.z : o.x) + dpp<ROR8>(b3 ? o.x : o.z), u1 = (b3 ? o.w : o.y) + dpp<ROR8>(b3 ? o.y : o.w);
      float w = (b2_ ? u1 : u0) + dpp<HMIR>(b2_ ? u0 : u1);
      w += dpp<XOR2>(w); w += dpp<XOR1>(w);
      w *= inv;
      const u32 od = pk_bf16(w, w);
      const v4u am[6] = {amask(row_bcast<0>(hd), msk), amask(row_bcast<8>(hd), msk),
                         amask(row_bcast<0>(od), msk), amask(row_bcast<4>(od), msk), amask(row_bcast<8>(od), msk), amask(row_bcast<12>(od), msk)};
      f32x4 y[3];
#pragma unroll
      for (int k = 0; k < 3; ++k) y[k] = (f32x4){0.f, 0.f, 0.f, 0.f};
#pragma unroll
      for (int s = 0; s < 6; ++s)
#pragma unroll
        for (int k = 0; k < 3; ++k) y[k] = mfma(am[s], s < 2 ? b2[k][s] : bo[k][s - 2], y[k]);
      if (lane < 32) {
        st_dev(tag_pack(y[0], tg), rws, yo, yb);
        st_dev(tag_pack(y[1], tg), rws, yo, yb + 32 * 4096);
        if (wave == 0) st_dev(tag_pack(y[2], tg), rws, yo, yb + 64 * 4096);
      }
    }
    if (wave == 0 && b < 160) {
      __builtin_amdgcn_s_sleep(FDELAY);
      v4u gy[4];
#pragma unroll
      for (int k = 0; k < 4; ++k) gy[k] = ld_dev(rws, l16 + 1024 * k, WS_Y + b * 4096);
      for (int spin2 = 0;;) {
        u32 bad[4];
#pragma unroll
        for (int k = 0; k < 4; ++k) bad[k] = tag_acc(0u, gy[k], tg) & TAGM;
        if (__builtin_amdgcn_ballot_w64((bad[0] | bad[1] | bad[2] | bad[3]) != 0) == 0 || ++spin2 >= SPIN_CAP) break;
        MEMFENCE();
#pragma unroll
        for (int k = 0; k < 4; ++k) if (bad[k]) gy[k] = ld_dev(rws, l16 + 1024 * k, WS_Y + b * 4096);
      }
      const float r = row_sum16(xrow_sum4((untag4(gy[0]) + untag4(gy[1])) + (untag4(gy[2]) + untag4(gy[3]))));
      const u32 xw = g < 2 ? xr.x : xr.y;
      const float res = r + ((g & 1) ? hi_f(xw) : lo_f(xw));
      if (c == 0) __hip_atomic_store((GAS u16*)p.out + 4 * b + g, (u16)pk_bf16(res, res), __ATOMIC_RELAXED, __HIP_MEMORY_SCOPE_AGENT);
      if (b == 0 && lane == 0) __hip_atomic_store((GAS u64*)(p.ws + WS_EPOCH), epoch + 1, __ATOMIC_RELAXED, __HIP_MEMORY_SCOPE_AGENT);
    }
  } else {
    const int kw = wave - 2;
    const __amdgpu_buffer_rsrc_t rk = RSRC(p.kc, NH * SK * DH * 2);
    const __amdgpu_buffer_rsrc_t rv = RSRC(p.vc, NH * SK * DH * 2);
    const u32 kvs = (u32)((h * SK + (SK - WIN) + 128 * j + 32 * ((kw + h + j) & 3)) * 256);
    const u32 kvo = g * 256 + c * 16, rot = (2 * h + (j >> 1) + 2 * j) & 7;
    const u64 epoch = __hip_atomic_load((GAS u64*)(p.ws + WS_EPOCH), __ATOMIC_RELAXED, __HIP_MEMORY_SCOPE_AGENT);
    BAR();
    LAS u32* kt = cnt + 4 + kw * (32 * 68);
    LAS u32* kwr = kt + g * (4 * 68) + c * 4;
    v4u kv[16];
#pragma unroll
    for (int n = 0; n < 16; ++n) {
      if (n >= KVWIN) {
        if (n - KVWIN < 8) *(LAS v4u*)(kwr + (16 * ((n - KVWIN) >> 2) + ((n - KVWIN) & 3)) * 68) = kv[n - KVWIN];
        else asm volatile("" ::"v"(kv[n - KVWIN]));
        SB();
      }
      kv[n] = ld_stream(n < 8 ? rk : rv, kvo, kvs + ((n + rot) & 7) * 1024);
      SB();
    }
    if (lane == 0) lds_inc(cnt + 2);
#pragma unroll
    for (int n = 16 - KVWIN; n < 8; ++n) *(LAS v4u*)(kwr + (16 * (n >> 2) + (n & 3)) * 68) = kv[n];
    v4u kf[2][4];
#pragma unroll
    for (int t = 0; t < 2; ++t)
#pragma unroll
      for (int s4 = 0; s4 < 4; ++s4) kf[t][s4] = *(LAS v4u*)(kt + (16 * t + c) * 68 + s4 * 16 + g * 4);
    asm volatile("" : "+v"(kf[0][0]), "+v"(kf[0][1]), "+v"(kf[0][2]), "+v"(kf[0][3]), "+v"(kf[1][0]), "+v"(kf[1][1]), "+v"(kf[1][2]), "+v"(kf[1][3]));
    const Tag tg = make_tag(epoch + 1);
    const u32 gi = 2 * c + g;
    u32 pvo = lane < 32 ? (gi >> 2) * 80 + (gi & 3) * 16 : (lane - 32) * 80 + 64, pso = WS_A + (h * 32 + 4 * j + kw) * WA_P;
    asm volatile("" : "+v"(pvo), "+s"(pso));
    { int spin = 0; while (lds_ld(cnt) < 2 && ++spin < SPIN_CAP) __builtin_amdgcn_s_sleep(1); }
    MEMFENCE();
    {
      f32x4 d0 = {0.f, 0.f, 0.f, 0.f}, d1 = {0.f, 0.f, 0.f, 0.f};
      v4u qf[4];
#pragma unroll
      for (int s4 = 0; s4 < 4; ++s4) qf[s4] = *(LAS v4u*)(qb + 16 * s4 + 4 * g);
      SB();
#pragma unroll
      for (int s4 = 0; s4 < 4; ++s4) { d0 = mfma(kf[0][s4], qf[s4], d0); d1 = mfma(kf[1][s4], qf[s4], d1); }
      const bool b0 = c & 1, b1 = c & 2, b2 = c & 4;
      const float x0 = b0 ? d0.y : d0.x, x1 = b0 ? d0.w : d0.z, x2 = b0 ? d1.y : d1.x, x3 = b0 ? d1.w : d1.z;
      const float y0 = b1 ? x1 : x0, y1 = b1 ? x3 : x2;
      const float d = b2 ? y1 : y0;
      const float s = rbf(rbf(d) * 0.08838834764831845f);
      const float m = xrow_max(max_hmir(max_xor2(max_xor1(s))));
      const float pe = ex2((s - m) * LOG2E);
      const u32 pd = pk_bf16(pe, pe);
      const v4u am[8] = {amask(row_bcast<0>(pd), msk), amask(row_bcast<1>(pd), msk), amask(row_bcast<2>(pd), msk), amask(row_bcast<3>(pd), msk),
                         amask(row_bcast<4>(pd), msk), amask(row_bcast<5>(pd), msk), amask(row_bcast<6>(pd), msk), amask(row_bcast<7>(pd), msk)};
      float l = pe + dpp<XOR1>(pe); l += dpp<XOR2>(l); l += dpp<HMIR>(l);
      l = xrow_sum(l);
      SB();
      f32x4 o = {0.f, 0.f, 0.f, 0.f};
#pragma unroll
      for (int n = 0; n < 8; ++n) o = mfma(am[n], kv[8 + n], o);
      if (lane >= 32) { o.x = m; o.y = l; o.z = m; o.w = l; }
      if (lane < 40) st_dev(tag_pack(o, tg), rws, pvo, pso);
      MEMFENCE();
      if (lane == 0) lds_inc(cnt + 1);
    }
  }
}

extern "C" void kernel_launch(void* const* d_in, const int* in_sizes, int n_in, void* d_out, int out_size, void* d_ws, size_t ws_size, hipStream_t stream) {
  Params p;
  p.x = (const u16*)d_in[0]; p.kc = (const u16*)d_in[1]; p.vc = (const u16*)d_in[2]; p.wq = (const u16*)d_in[3]; p.wo = (const u16*)d_in[4];
  p.w1 = (const u16*)d_in[6]; p.w2 = (const u16*)d_in[7]; p.out = (u16*)d_out; p.ws = (unsigned char*)d_ws;
  fused_decode<<<256, 384, 0, stream>>>(p);
}
```

```cpp
#define AMPG 0
#include <hip/hip_runtime.h>
#include <hip/hip_bf16.h>

typedef unsigned short u16;
typedef unsigned int u32;
typedef unsigned long long u64;
typedef u32 v4u __attribute__((ext_vector_type(4)));
typedef u32 v2u __attribute__((ext_vector_type(2)));
typedef float f32x4 __attribute__((ext_vector_type(4)));
typedef __bf16 bf16x8 __attribute__((ext_vector_type(8)));
typedef __bf16 bf16x2 __attribute__((ext_vector_type(2)));
#define GAS __attribute__((address_space(1)))
#define LAS __attribute__((address_space(3)))

constexpr int KVWIN = 6;
constexpr int QDELAY = 28, ADELAY = 20, FDELAY = 28;
constexpr int AUX_NT = 2, AUX_SC1 = 16;

constexpr int D_ = 640, NH = 32, SK = 4096, DH = 128, WIN = 1024, FF = 1728;
constexpr u32 WQ_P = 512, WH_P = 256, WA_P = 640;
constexpr u32 WS_EPOCH = 0;
constexpr u32 WS_Q = 4096;
constexpr u32 WS_H = WS_Q + 32 * 16 * WQ_P;
constexpr u32 WS_A = WS_H + 32 * 16 * WH_P;
constexpr u32 WS_Y = WS_A + 32 * 32 * WA_P;
constexpr u32 WS_END = WS_Y + 256 * 160 * 16;
constexpr u32 TAGM = 0x3FFu;
constexpr int SPIN_CAP = 200000;
constexpr float LOG2E = 1.4426950408889634f;

#define R4(x) x x x x
#define R64(x) R4(R4(R4(x)))
#define CAS __attribute__((address_space(4)))
struct Params {
  const u16* x; const u16* wq; unsigned char* ws; const u16* kc; const u16* vc; const u16* w1; const u16* wo; const u16* w2; u16* out;
};

#define SB() __builtin_amdgcn_sched_barrier(0)
#define BAR() do { SB(); asm volatile("s_waitcnt lgkmcnt(0)\n\ts_barrier" ::: "memory"); SB(); } while (0)
#define MEMFENCE() asm volatile("" ::: "memory")


__device__ __forceinline__ float asf(u32 x) { return __builtin_bit_cast(float, x); }
__device__ __forceinline__ u32 asu(float x) { return __builtin_bit_cast(u32, x); }
constexpr int ROR8 = 0x128, ROR4 = 0x124, ROR2 = 0x122, ROR1 = 0x121, HMIR = 0x141, XOR2 = 0x4E, XOR1 = 0xB1;
template <int CTRL> __device__ __forceinline__ float dpp(float x) { return asf(__builtin_amdgcn_mov_dpp(asu(x), CTRL, 0xf, 0xf, true)); }
template <int L> __device__ __forceinline__ u32 row_bcast(u32 x) { return __builtin_amdgcn_mov_dpp(x, 0x150 + L, 0xf, 0xf, true); }
__device__ __forceinline__ float row_sum16(float x) { x += dpp<ROR8>(x); x += dpp<ROR4>(x); x += dpp<ROR2>(x); x += dpp<ROR1>(x); return x; }
__device__ __forceinline__ float vmax(float a, float b) { float r; asm("v_max_f32 %0, %1, %2" : "=v"(r) : "v"(a), "v"(b)); return r; }
#define DPP_MAX(name, ctrl) __device__ __forceinline__ float name(float x) { float r; asm("s_nop 1\n\tv_max_f32_dpp %0, %1, %1 " ctrl " row_mask:0xf bank_mask:0xf" : "=&v"(r) : "v"(x)); return r; }
DPP_MAX(max_ror8, "row_ror:8") DPP_MAX(max_ror4, "row_ror:4") DPP_MAX(max_ror2, "row_ror:2") DPP_MAX(max_ror1, "row_ror:1")
DPP_MAX(max_hmir, "row_half_mirror") DPP_MAX(max_xor2, "quad_perm:[2,3,0,1]") DPP_MAX(max_xor1, "quad_perm:[1,0,3,2]")
__device__ __forceinline__ float xrow_sum(float x) {
  auto s = __builtin_amdgcn_permlane16_swap(asu(x), asu(x), false, false);
  x = asf(s[0]) + asf(s[1]);
  auto t = __builtin_amdgcn_permlane32_swap(asu(x), asu(x), false, false);
  return asf(t[0]) + asf(t[1]);
}
__device__ __forceinline__ float xrow_max(float x) {
  auto s = __builtin_amdgcn_permlane16_swap(asu(x), asu(x), false, false);
  x = vmax(asf(s[0]), asf(s[1]));
  auto t = __builtin_amdgcn_permlane32_swap(asu(x), asu(x), false, false);
  return vmax(asf(t[0]), asf(t[1]));
}
__device__ __forceinline__ float xrow_sum4(f32x4 a) {
  auto s0 = __builtin_amdgcn_permlane32_swap(asu(a.x), asu(a.z), false, false);
  auto s1 = __builtin_amdgcn_permlane32_swap(asu(a.y), asu(a.w), false, false);
  auto s2 = __builtin_amdgcn_permlane16_swap(asu(asf(s0[0]) + asf(s0[1])), asu(asf(s1[0]) + asf(s1[1])), false, false);
  return asf(s2[0]) + asf(s2[1]);
}
__device__ __forceinline__ u32 pk_bf16(float lo, float hi) {
  bf16x2 r = {(__bf16)lo, (__bf16)hi};
  return __builtin_bit_cast(u32, r);
}
__device__ __forceinline__ float rbf(float x) { return asf(pk_bf16(x, x) << 16); }
__device__ __forceinline__ float lo_f(u32 w) { return asf(w << 16); }
__device__ __forceinline__ float hi_f(u32 w) { return asf(w & 0xffff0000u); }
__device__ __forceinline__ float rcp(float x) { return __builtin_amdgcn_rcpf(x); }
__device__ __forceinline__ float ex2(float x) { return __builtin_amdgcn_exp2f(x); }
__device__ __forceinline__ float dot2(u32 a, u32 b, float c) {
  return __builtin_amdgcn_fdot2_f32_bf16(__builtin_bit_cast(bf16x2, a), __builtin_bit_cast(bf16x2, b), c, false);
}
__device__ __forceinline__ f32x4 mfma(v4u a, v4u b, f32x4 c) {
  return __builtin_amdgcn_mfma_f32_16x16x32_bf16(__builtin_bit_cast(bf16x8, a), __builtin_bit_cast(bf16x8, b), c, 0, 0, 0);
}
__device__ __forceinline__ v4u amask(u32 dup, const v4u& m) { v4u r = {dup & m.x, dup & m.y, dup & m.z, dup & m.w}; return r; }

struct Tag { u32 f0, f1, f2, f3, keep; };
__device__ __forceinline__ Tag make_tag(u64 T) {
  const u32 tl = __builtin_amdgcn_readfirstlane((u32)T), th = __builtin_amdgcn_readfirstlane((u32)(T >> 32));
  const u64 Tu = ((u64)th << 32) | tl;
  Tag tg; tg.f0 = tl & TAGM; tg.f1 = ((u32)(Tu >> 10) + tl) & TAGM; tg.f2 = ((u32)(Tu >> 20) + tl) & TAGM; tg.f3 = ((u32)(Tu >> 30) + tl) & TAGM;
  tg.keep = ~TAGM;
  asm volatile("" : "+s"(tg.f0), "+s"(tg.f1), "+s"(tg.f2), "+s"(tg.f3), "+v"(tg.keep));
  return tg;
}
__device__ __forceinline__ v4u tag_pack(f32x4 v, const Tag& t) {
  v4u r = {(asu(v.x) & t.keep) | t.f0, (asu(v.y) & t.keep) | t.f1, (asu(v.z) & t.keep) | t.f2, (asu(v.w) & t.keep) | t.f3};
  return r;
}
__device__ __forceinline__ u32 tag_acc(u32 acc, v4u g, const Tag& t) {
  acc = __builtin_amdgcn_bitop3_b32(g.x, t.f0, acc, 0xBE); acc = __builtin_amdgcn_bitop3_b32(g.y, t.f1, acc, 0xBE);
  acc = __builtin_amdgcn_bitop3_b32(g.z, t.f2, acc, 0xBE); acc = __builtin_amdgcn_bitop3_b32(g.w, t.f3, acc, 0xBE);
  return acc;
}
__device__ __forceinline__ bool any_bad(u32 acc) { return __builtin_amdgcn_ballot_w64((acc & TAGM) != 0) != 0; }
__device__ __forceinline__ float untag(u32 w) { return asf(w & ~TAGM); }
__device__ __forceinline__ f32x4 untag4(v4u g) { f32x4 r = {untag(g.x), untag(g.y), untag(g.z), untag(g.w)}; return r; }

#define RSRC(ptr, bytes) __builtin_amdgcn_make_buffer_rsrc((void*)(ptr), 0, (int)(bytes), 0x00020000)
__device__ __forceinline__ v4u ld_stream(__amdgpu_buffer_rsrc_t r, u32 voff, u32 soff) { return __builtin_amdgcn_raw_buffer_load_b128(r, voff, soff, AUX_NT); }
__device__ __forceinline__ v4u ld_dev(__amdgpu_buffer_rsrc_t r, u32 voff, u32 soff) { return __builtin_amdgcn_raw_buffer_load_b128(r, voff, soff, AUX_SC1); }
__device__ __forceinline__ void st_dev(v4u v, __amdgpu_buffer_rsrc_t r, u32 voff, u32 soff) { __builtin_amdgcn_raw_buffer_store_b128(v, r, voff, soff, AUX_SC1); }
__device__ __forceinline__ u32 lds_ld(LAS u32* p) { return __hip_atomic_load(p, __ATOMIC_RELAXED, __HIP_MEMORY_SCOPE_WORKGROUP); }
__device__ __forceinline__ void lds_inc(LAS u32* p) { asm volatile("ds_add_u32 %0, %1" ::"v"((u32)(size_t)p), "v"(1u) : "memory"); }

__device__ __forceinline__ void ln_slice(v4u xa, v4u xb, u32 xo, int lane, LAS u32* xs) {
  float xv[16];
#pragma unroll
  for (int k = 0; k < 4; ++k) { xv[2 * k] = lo_f(xa[k]); xv[2 * k + 1] = hi_f(xa[k]); xv[8 + 2 * k] = lo_f(xb[k]); xv[9 + 2 * k] = hi_f(xb[k]); }
  float s0 = 0.f, s1 = 0.f;
#pragma unroll
  for (int k = 0; k < 8; ++k) { s0 += xv[k]; s1 += xv[8 + k]; }
  const float mean = xrow_sum(row_sum16(s0 + s1)) * (1.f / D_);
  float v1 = 0.f, v2 = 0.f;
#pragma unroll
  for (int k = 0; k < 8; ++k) { const float d1 = xv[k] - mean, d2 = xv[8 + k] - mean; v1 = fmaf(d1, d1, v1); v2 = fmaf(d2, d2, v2); }
  const float var = xrow_sum(row_sum16(lane < 16 ? v1 + v2 : v1)) * (1.f / D_);
  const float mb = rbf(mean), inv = rcp(rbf(__builtin_amdgcn_sqrtf(rbf(rbf(var) + 1e-5f))));
  const float o0 = rbf(lo_f(xo) - mb) * inv, o1 = rbf(hi_f(xo) - mb) * inv;
  v2u o = {pk_bf16(o0, o0), pk_bf16(o1, o1)};
  if (lane < 20) *(LAS v2u*)(xs + 2 * lane) = o;
  asm volatile("s_waitcnt lgkmcnt(0)" ::: "memory");
}

__global__ void __launch_bounds__(384, 2) fused_decode(Params p_) {
  u64 tf_, tb_, ta_; asm volatile("s_memrealtime %0\n\ts_waitcnt lgkmcnt(0)" : "=s"(tf_)::"memory"); SB();
  { float a_ = 1.f; asm volatile(R64("v_add_f32_e64 %0, %0, %0\n\t") R64("v_add_f32_e64 %0, %0, %0\n\t") : "+v"(a_)); } SB();
  asm volatile("s_memrealtime %0\n\ts_waitcnt lgkmcnt(0)" : "=s"(tb_)::"memory"); SB();
  const CAS Params* pp_ = (const CAS Params*)__builtin_amdgcn_kernarg_segment_ptr(); asm volatile("" : "+s"(pp_));
  Params p; p.x = pp_->x; p.wq = pp_->wq; p.ws = pp_->ws; p.kc = pp_->kc; p.vc = pp_->vc; p.w1 = pp_->w1; p.wo = pp_->wo; p.w2 = pp_->w2; p.out = pp_->out;
  asm volatile("" : "+s"(p.x), "+s"(p.kc), "+s"(p.vc), "+s"(p.wq), "+s"(p.wo), "+s"(p.w1), "+s"(p.w2), "+s"(p.out), "+s"(p.ws));
  asm volatile("s_memrealtime %0\n\ts_waitcnt lgkmcnt(0)" : "=s"(ta_)::"memory"); SB();

  __shared__ __attribute__((aligned(16))) u32 lds[80 + 64 + 4 + 4 * 32 * 68];
  LAS u32* xs = (LAS u32*)lds;
  LAS u32* qb = xs + 80;
  LAS u32* cnt = qb + 64;

  const int tid = threadIdx.x, lane = tid & 63;
  const int wave = __builtin_amdgcn_readfirstlane(tid >> 6);
  const int b = blockIdx.x, h = b >> 3, j = b & 7;
  const int c = lane & 15, g = lane >> 4;
  const bool ffn = h < 27;
  v4u msk;
  {
    const u32 half = (c & 1) ? 0xffff0000u : 0x0000ffffu; const int r = (c & 7) >> 1;
    msk.x = r == 0 ? half : 0u; msk.y = r == 1 ? half : 0u; msk.z = r == 2 ? half : 0u; msk.w = r == 3 ? half : 0u;
  }
  u32 l16 = lane * 16, xoo = (lane < 20 ? lane : 0) * 4, wqo = g * (10 * 8192) + c * 16;
  asm volatile("" : "+v"(msk.x), "+v"(msk.y), "+v"(msk.z), "+v"(msk.w), "+v"(l16), "+v"(xoo), "+v"(wqo));
  SB();
  asm volatile("" : "+s"(p.x), "+s"(p.kc), "+s"(p.vc), "+s"(p.wq), "+s"(p.wo), "+s"(p.w1), "+s"(p.w2), "+s"(p.out), "+s"(p.ws));
  const __amdgpu_buffer_rsrc_t rws = RSRC(p.ws, WS_END);

  if (__builtin_expect(wave < 2, 1)) {
    __builtin_amdgcn_s_setprio(3);
    const __amdgpu_buffer_rsrc_t rx = RSRC(p.x, D_ * 2);
    const __amdgpu_buffer_rsrc_t rwq = RSRC(p.wq, D_ * 4096 * 2);
    const int row0 = 80 * j + 40 * wave;
    const v4u xa = __builtin_amdgcn_raw_buffer_load_b128(rx, l16, 0, 0);
    const v4u xb = __builtin_amdgcn_raw_buffer_load_b128(rx, l16 + 1024, 0, 0);
    const u32 xo = __builtin_amdgcn_raw_buffer_load_b32(rx, xoo, row0 * 2, 0);
    const u64 epoch = __hip_atomic_load((GAS u64*)(p.ws + WS_EPOCH), __ATOMIC_RELAXED, __HIP_MEMORY_SCOPE_AGENT);
    SB();
    v4u aq[10];
#pragma unroll
    for (int s = 0; s < 10; ++s) aq[s] = ld_stream(rwq, wqo, (row0 + s) * 8192 + h * 256);
    SB();
    const v2u xr = __builtin_amdgcn_raw_buffer_load_b64(rx, 0, (b < 160 ? b : 0) * 8, 0);
    if (lane < 4) cnt[lane] = 0;
    BAR();
    ln_slice(xa, xb, xo, lane, xs + 40 * wave);
    u32 xd[10];
#pragma unroll
    for (int s2 = 0; s2 < 5; ++s2) { const v2u t = *(LAS v2u*)(xs + 40 * wave + 10 * g + 2 * s2); xd[2 * s2] = t.x; xd[2 * s2 + 1] = t.y; }
    const Tag tg = make_tag(epoch + 1);
    u32 gvo = (2 * c + g) * 16, qso = WS_Q + (h * 16 + 2 * j + wave) * WQ_P;
    asm volatile("" : "+v"(gvo), "+s"(qso));
    f32x4 dq = {0.f, 0.f, 0.f, 0.f};
#pragma unroll
    for (int s = 0; s < 10; ++s) dq = mfma(amask(xd[s], msk), aq[s], dq);
    if (lane < 32) st_dev(tag_pack(dq, tg), rws, gvo, qso);
    __builtin_amdgcn_s_sleep(QDELAY);
    {
      const u32 qo = g * WQ_P + (16 * wave + c) * 16, qbase = WS_Q + h * 16 * WQ_P;
      v4u gq[4]; int spin = 0;
      do {
        MEMFENCE();
#pragma unroll
        for (int k = 0; k < 4; ++k) gq[k] = ld_dev(rws, qo, qbase + k * 4 * WQ_P);
      } while (any_bad(tag_acc(tag_acc(tag_acc(tag_acc(0u, gq[0], tg), gq[1], tg), gq[2], tg), gq[3], tg)) && ++spin < SPIN_CAP);
      const float r = xrow_sum4((untag4(gq[0]) + untag4(gq[1])) + (untag4(gq[2]) + untag4(gq[3])));
      ((LAS u16*)qb)[4 * (16 * wave + c) + g] = (u16)pk_bf16(r, r);
      MEMFENCE();
      if (lane == 0) lds_inc(cnt);
    }
    const __amdgpu_buffer_rsrc_t rw1 = RSRC(p.w1, ffn ? D_ * FF * 2 : 0);
    v4u ah[5];
#pragma unroll
    for (int s = 0; s < 5; ++s) ah[s] = ld_stream(rw1, (10 * g + 5 * (c >> 3)) * (FF * 2) + (c & 7) * 16, (row0 + s) * (FF * 2) + h * 128);
    SB();
    { int spin = 0; while (lds_ld(cnt + 2) < 4 && ++spin < SPIN_CAP) __builtin_amdgcn_s_sleep(1); }
    const __amdgpu_buffer_rsrc_t rwo = RSRC(p.wo, 4096 * D_ * 2);
    const __amdgpu_buffer_rsrc_t rw2 = RSRC(p.w2, ffn ? FF * D_ * 2 : 0);
    v4u bo[3][4], b2[3][2];
#pragma unroll
    for (int k = 0; k < 3; ++k) {
      const bool valid = k < 2 || wave == 0;
      const u32 so = valid ? (h * 128 + 16 * j) * 1280 + (3 * wave + k) * 256 : 0x70000000u, s2 = valid ? (h * 64 + 8 * j) * 1280 + (3 * wave + k) * 256 : 0x70000000u;
#pragma unroll
      for (int s = 0; s < 2; ++s) b2[k][s] = ld_stream(rw2, (2 * g + s) * 1280 + c * 16, s2);
#pragma unroll
      for (int s = 0; s < 4; ++s) bo[k][s] = ld_stream(rwo, (4 * g + s) * 1280 + c * 16, so);
    }
    SB();
    f32x4 dh = {0.f, 0.f, 0.f, 0.f};
#pragma unroll
    for (int s = 0; s < 5; ++s) dh = mfma(amask((c >> 3) ? xd[5 + s] : xd[s], msk), ah[s], dh);
#pragma unroll
    for (int k = 0; k < 4; ++k) {
      auto t = __builtin_amdgcn_permlane32_swap(asu(dh[k]), asu(dh[k]), false, false);
      dh[k] = asf(t[0]) + dpp<ROR8>(asf(t[1]));
    }
    if (c < 8 && g < 2) st_dev(tag_pack(dh, tg), rws, (2 * c + g) * 16, WS_H + (h * 16 + 2 * j + wave) * WH_P);
    { int spin = 0; while (lds_ld(cnt + 1) < 4 && ++spin < SPIN_CAP) __builtin_amdgcn_s_sleep(1); }
    __builtin_amdgcn_s_sleep(ADELAY);
    const u32 aoff = c * WA_P + j * 80 + g * 16, moff = c * WA_P + j * 80 + 64, abase = WS_A + h * 32 * WA_P;
    const u32 hoff = c * WH_P + (2 * j + (g >> 1)) * 16, hbase = WS_H + h * 16 * WH_P;
    u32 yo = (2 * c + g) * 4096, yb = WS_Y + wave * 96 * 4096 + b * 16;
    asm volatile("" : "+v"(yo), "+s"(yb));
    v4u go0, go1, gm0, gm1, gh; int spin = 0;
    do {
      MEMFENCE();
      gh = ld_dev(rws, hoff, hbase); gm0 = ld_dev(rws, moff, abase); gm1 = ld_dev(rws, moff, abase + 16 * WA_P);
      go0 = ld_dev(rws, aoff, abase); go1 = ld_dev(rws, aoff, abase + 16 * WA_P);
    } while (any_bad(tag_acc(tag_acc(tag_acc(tag_acc(tag_acc(0u, gh, tg), gm0, tg), gm1, tg), go0, tg), go1, tg)) && ++spin < SPIN_CAP);
    {
      const bool b3 = c & 8, b2_ = c & 4;
      const float h0 = untag((g & 1) ? gh.z : gh.x), h1 = untag((g & 1) ? gh.w : gh.y);
      float hs = (b3 ? h1 : h0) + dpp<ROR8>(b3 ? h0 : h1);
      hs += dpp<HMIR>(hs); hs += dpp<XOR2>(hs); hs += dpp<XOR1>(hs);
      const float ht = rbf(hs), ha = ht * rcp(1.f + ex2(-ht * LOG2E));
      const u32 hd = pk_bf16(ha, ha);
      const float m0 = untag(gm0.x), l0 = untag(gm0.y), m1 = untag(gm1.x), l1 = untag(gm1.y);
      const float nM = -max_ror1(max_ror2(max_ror4(max_ror8(vmax(m0, m1))))) * LOG2E;
      const float f0 = ex2(fmaf(m0, LOG2E, nM)), f1 = ex2(fmaf(m1, LOG2E, nM));
      const float inv = rcp(row_sum16(fmaf(l0, f0, l1 * f1)));
      const f32x4 o = untag4(go0) * f0 + untag4(go1) * f1;
      const float u0 = (b3 ? o.z : o.x) + dpp<ROR8>(b3 ? o.x : o.z), u1 = (b3 ? o.w : o.y) + dpp<ROR8>(b3 ? o.y : o.w);
      float w = (b2_ ? u1 : u0) + dpp<HMIR>(b2_ ? u0 : u1);
      w += dpp<XOR2>(w); w += dpp<XOR1>(w);
      w *= inv;
      const u32 od = pk_bf16(w, w);
      const v4u am[6] = {amask(row_bcast<0>(hd), msk), amask(row_bcast<8>(hd), msk),
                         amask(row_bcast<0>(od), msk), amask(row_bcast<4>(od), msk), amask(row_bcast<8>(od), msk), amask(row_bcast<12>(od), msk)};
      f32x4 y[3];
#pragma unroll
      for (int k = 0; k < 3; ++k) y[k] = (f32x4){0.f, 0.f, 0.f, 0.f};
#pragma unroll
      for (int s = 0; s < 6; ++s)
#pragma unroll
        for (int k = 0; k < 3; ++k) y[k] = mfma(am[s], s < 2 ? b2[k][s] : bo[k][s - 2], y[k]);
      if (lane < 32) {
        st_dev(tag_pack(y[0], tg), rws, yo, yb);
        st_dev(tag_pack(y[1], tg), rws, yo, yb + 32 * 4096);
        if (wave == 0) st_dev(tag_pack(y[2], tg), rws, yo, yb + 64 * 4096);
      }
    }
    if (wave == 0 && b < 160) {
      __builtin_amdgcn_s_sleep(FDELAY);
      v4u gy[4];
#pragma unroll
      for (int k = 0; k < 4; ++k) gy[k] = ld_dev(rws, l16 + 1024 * k, WS_Y + b * 4096);
      for (int spin2 = 0;;) {
        u32 bad[4];
#pragma unroll
        for (int k = 0; k < 4; ++k) bad[k] = tag_acc(0u, gy[k], tg) & TAGM;
        if (__builtin_amdgcn_ballot_w64((bad[0] | bad[1] | bad[2] | bad[3]) != 0) == 0 || ++spin2 >= SPIN_CAP) break;
        MEMFENCE();
#pragma unroll
        for (int k = 0; k < 4; ++k) if (bad[k]) gy[k] = ld_dev(rws, l16 + 1024 * k, WS_Y + b * 4096);
      }
      const float r = row_sum16(xrow_sum4((untag4(gy[0]) + untag4(gy[1])) + (untag4(gy[2]) + untag4(gy[3]))));
      const u32 xw = g < 2 ? xr.x : xr.y;
      const float res = r + ((g & 1) ? hi_f(xw) : lo_f(xw));
      if (c == 0) __hip_atomic_store((GAS u16*)p.out + 4 * b + g, (u16)pk_bf16(res, res), __ATOMIC_RELAXED, __HIP_MEMORY_SCOPE_AGENT);
      if (b == 0 && lane == 0) __hip_atomic_store((GAS u64*)(p.ws + WS_EPOCH), epoch + 1, __ATOMIC_RELAXED, __HIP_MEMORY_SCOPE_AGENT);
      if (b == 0) { u64 now; asm volatile("s_memrealtime %0\n\ts_waitcnt lgkmcnt(0)" : "=s"(now)::"memory"); const u32 iv = (u32)(ta_ - tb_); const u64 tgt = now + (u64)AMPG * (iv > 100000 ? 0 : iv); int guard = 0; do { asm volatile("s_memrealtime %0\n\ts_waitcnt lgkmcnt(0)" : "=s"(now)::"memory"); } while (now < tgt && ++guard < 1000000); }
    }
  } else {
    const int kw = wave - 2;
    const __amdgpu_buffer_rsrc_t rk = RSRC(p.kc, NH * SK * DH * 2);
    const __amdgpu_buffer_rsrc_t rv = RSRC(p.vc, NH * SK * DH * 2);
    const u32 kvs = (u32)((h * SK + (SK - WIN) + 128 * j + 32 * ((kw + h + j) & 3)) * 256);
    const u32 kvo = g * 256 + c * 16, rot = (2 * h + (j >> 1) + 2 * j) & 7;
    const u64 epoch = __hip_atomic_load((GAS u64*)(p.ws + WS_EPOCH), __ATOMIC_RELAXED, __HIP_MEMORY_SCOPE_AGENT);
    BAR();
    LAS u32* kt = cnt + 4 + kw * (32 * 68);
    LAS u32* kwr = kt + g * (4 * 68) + c * 4;
    v4u kv[16];
#pragma unroll
    for (int n = 0; n < 16; ++n) {
      if (n >= KVWIN) {
        if (n - KVWIN < 8) *(LAS v4u*)(kwr + (16 * ((n - KVWIN) >> 2) + ((n - KVWIN) & 3)) * 68) = kv[n - KVWIN];
        else asm volatile("" ::"v"(kv[n - KVWIN]));
        SB();
      }
      kv[n] = ld_stream(n < 8 ? rk : rv, kvo, kvs + ((n + rot) & 7) * 1024);
      SB();
    }
    if (lane == 0) lds_inc(cnt + 2);
#pragma unroll
    for (int n = 16 - KVWIN; n < 8; ++n) *(LAS v4u*)(kwr + (16 * (n >> 2) + (n & 3)) * 68) = kv[n];
    v4u kf[2][4];
#pragma unroll
    for (int t = 0; t < 2; ++t)
#pragma unroll
      for (int s4 = 0; s4 < 4; ++s4) kf[t][s4] = *(LAS v4u*)(kt + (16 * t + c) * 68 + s4 * 16 + g * 4);
    asm volatile("" : "+v"(kf[0][0]), "+v"(kf[0][1]), "+v"(kf[0][2]), "+v"(kf[0][3]), "+v"(kf[1][0]), "+v"(kf[1][1]), "+v"(kf[1][2]), "+v"(kf[1][3]));
    const Tag tg = make_tag(epoch + 1);
    const u32 gi = 2 * c + g;
    u32 pvo = lane < 32 ? (gi >> 2) * 80 + (gi & 3) * 16 : (lane - 32) * 80 + 64, pso = WS_A + (h * 32 + 4 * j + kw) * WA_P;
    asm volatile("" : "+v"(pvo), "+s"(pso));
    { int spin = 0; while (lds_ld(cnt) < 2 && ++spin < SPIN_CAP) __builtin_amdgcn_s_sleep(1); }
    MEMFENCE();
    {
      f32x4 d0 = {0.f, 0.f, 0.f, 0.f}, d1 = {0.f, 0.f, 0.f, 0.f};
      v4u qf[4];
#pragma unroll
      for (int s4 = 0; s4 < 4; ++s4) qf[s4] = *(LAS v4u*)(qb + 16 * s4 + 4 * g);
      SB();
#pragma unroll
      for (int s4 = 0; s4 < 4; ++s4) { d0 = mfma(kf[0][s4], qf[s4], d0); d1 = mfma(kf[1][s4], qf[s4], d1); }
      const bool b0 = c & 1, b1 = c & 2, b2 = c & 4;
      const float x0 = b0 ? d0.y : d0.x, x1 = b0 ? d0.w : d0.z, x2 = b0 ? d1.y : d1.x, x3 = b0 ? d1.w : d1.z;
      const float y0 = b1 ? x1 : x0, y1 = b1 ? x3 : x2;
      const float d = b2 ? y1 : y0;
      const float s = rbf(rbf(d) * 0.08838834764831845f);
      const float m = xrow_max(max_hmir(max_xor2(max_xor1(s))));
      const float pe = ex2((s - m) * LOG2E);
      const u32 pd = pk_bf16(pe, pe);
      const v4u am[8] = {amask(row_bcast<0>(pd), msk), amask(row_bcast<1>(pd), msk), amask(row_bcast<2>(pd), msk), amask(row_bcast<3>(pd), msk),
                         amask(row_bcast<4>(pd), msk), amask(row_bcast<5>(pd), msk), amask(row_bcast<6>(pd), msk), amask(row_bcast<7>(pd), msk)};
      float l = pe + dpp<XOR1>(pe); l += dpp<XOR2>(l); l += dpp<HMIR>(l);
      l = xrow_sum(l);
      SB();
      f32x4 o = {0.f, 0.f, 0.f, 0.f};
#pragma unroll
      for (int n = 0; n < 8; ++n) o = mfma(am[n], kv[8 + n], o);
      if (lane >= 32) { o.x = m; o.y = l; o.z = m; o.w = l; }
      if (lane < 40) st_dev(tag_pack(o, tg), rws, pvo, pso);
      MEMFENCE();
      if (lane == 0) lds_inc(cnt + 1);
    }
  }
}

extern "C" void kernel_launch(void* const* d_in, const int* in_sizes, int n_in, void* d_out, int out_size, void* d_ws, size_t ws_size, hipStream_t stream) {
  Params p;
  p.x = (const u16*)d_in[0]; p.kc = (const u16*)d_in[1]; p.vc = (const u16*)d_in[2]; p.wq = (const u16*)d_in[3]; p.wo = (const u16*)d_in[4];
  p.w1 = (const u16*)d_in[6]; p.w2 = (const u16*)d_in[7]; p.out = (u16*)d_out; p.ws = (unsigned char*)d_ws;
  fused_decode<<<256, 384, 0, stream>>>(p);
}
```

```cpp
#include <hip/hip_runtime.h>
#include <hip/hip_bf16.h>

typedef unsigned short u16;
typedef unsigned int u32;
typedef unsigned long long u64;
typedef u32 v4u __attribute__((ext_vector_type(4)));
typedef u32 v2u __attribute__((ext_vector_type(2)));
typedef float f32x4 __attribute__((ext_vector_type(4)));
typedef __bf16 bf16x8 __attribute__((ext_vector_type(8)));
typedef __bf16 bf16x2 __attribute__((ext_vector_type(2)));
#define GAS __attribute__((address_space(1)))
#define LAS __attribute__((address_space(3)))

constexpr int KVWIN = 6;
constexpr int OEARLY = 5;
constexpr int QDELAY = 27, ADELAY = 20, FDELAY = 26;
constexpr int AUX_NT = 2, AUX_SC1 = 16;

constexpr int D_ = 640, NH = 32, SK = 4096, DH = 128, WIN = 1024, FF = 1728;
constexpr u32 WQ_P = 512, WH_P = 256, WA_P = 640;
constexpr u32 WS_EPOCH = 0;
constexpr u32 WS_Q = 4096;
constexpr u32 WS_H = WS_Q + 32 * 16 * WQ_P;
constexpr u32 WS_A = WS_H + 32 * 16 * WH_P;
constexpr u32 WS_Y = WS_A + 32 * 32 * WA_P;
constexpr u32 WS_END = WS_Y + 256 * 160 * 16;
constexpr u32 TAGM = 0x3FFu;
constexpr int SPIN_CAP = 200000;
constexpr float LOG2E = 1.4426950408889634f;

struct Params {
  const u16* x; const u16* wq; unsigned char* ws; const u16* kc; const u16* vc; const u16* w1; const u16* wo; const u16* w2; u16* out;
};

#define SB() __builtin_amdgcn_sched_barrier(0)
#define BAR() do { SB(); asm volatile("s_waitcnt lgkmcnt(0)\n\ts_barrier" ::: "memory"); SB(); } while (0)
#define MEMFENCE() asm volatile("" ::: "memory")

__device__ __forceinline__ float asf(u32 x) { return __builtin_bit_cast(float, x); }
__device__ __forceinline__ u32 asu(float x) { return __builtin_bit_cast(u32, x); }
constexpr int ROR8 = 0x128, ROR4 = 0x124, ROR2 = 0x122, ROR1 = 0x121, HMIR = 0x141, XOR2 = 0x4E, XOR1 = 0xB1;
template <int CTRL> __device__ __forceinline__ float dpp(float x) { return asf(__builtin_amdgcn_mov_dpp(asu(x), CTRL, 0xf, 0xf, true)); }
template <int L> __device__ __forceinline__ u32 row_bcast(u32 x) { return __builtin_amdgcn_mov_dpp(x, 0x150 + L, 0xf, 0xf, true); }
__device__ __forceinline__ float row_sum16(float x) { x += dpp<ROR8>(x); x += dpp<ROR4>(x); x += dpp<ROR2>(x); x += dpp<ROR1>(x); return x; }
__device__ __forceinline__ float vmax(float a, float b) { float r; asm("v_max_f32 %0, %1, %2" : "=v"(r) : "v"(a), "v"(b)); return r; }
#define DPP_MAX(name, ctrl) __device__ __forceinline__ float name(float x) { float r; asm("s_nop 1\n\tv_max_f32_dpp %0, %1, %1 " ctrl " row_mask:0xf bank_mask:0xf" : "=&v"(r) : "v"(x)); return r; }
DPP_MAX(max_ror8, "row_ror:8") DPP_MAX(max_ror4, "row_ror:4") DPP_MAX(max_ror2, "row_ror:2") DPP_MAX(max_ror1, "row_ror:1")
DPP_MAX(max_hmir, "row_half_mirror") DPP_MAX(max_xor2, "quad_perm:[2,3,0,1]") DPP_MAX(max_xor1, "quad_perm:[1,0,3,2]")
__device__ __forceinline__ float xrow_sum(float x) {
  auto s = __builtin_amdgcn_permlane16_swap(asu(x), asu(x), false, false);
  x = asf(s[0]) + asf(s[1]);
  auto t = __builtin_amdgcn_permlane32_swap(asu(x), asu(x), false, false);
  return asf(t[0]) + asf(t[1]);
}
__device__ __forceinline__ float xrow_max(float x) {
  auto s = __builtin_amdgcn_permlane16_swap(asu(x), asu(x), false, false);
  x = vmax(asf(s[0]), asf(s[1]));
  auto t = __builtin_amdgcn_permlane32_swap(asu(x), asu(x), false, false);
  return vmax(asf(t[0]), asf(t[1]));
}
__device__ __forceinline__ float xrow_sum4(f32x4 a) {
  auto s0 = __builtin_amdgcn_permlane32_swap(asu(a.x), asu(a.z), false, false);
  auto s1 = __builtin_amdgcn_permlane32_swap(asu(a.y), asu(a.w), false, false);
  auto s2 = __builtin_amdgcn_permlane16_swap(asu(asf(s0[0]) + asf(s0[1])), asu(asf(s1[0]) + asf(s1[1])), false, false);
  return asf(s2[0]) + asf(s2[1]);
}
__device__ __forceinline__ u32 pk_bf16(float lo, float hi) {
  bf16x2 r = {(__bf16)lo, (__bf16)hi};
  return __builtin_bit_cast(u32, r);
}
__device__ __forceinline__ float rbf(float x) { return asf(pk_bf16(x, x) << 16); }
__device__ __forceinline__ float lo_f(u32 w) { return asf(w << 16); }
__device__ __forceinline__ float hi_f(u32 w) { return asf(w & 0xffff0000u); }
__device__ __forceinline__ float rcp(float x) { return __builtin_amdgcn_rcpf(x); }
__device__ __forceinline__ float ex2(float x) { return __builtin_amdgcn_exp2f(x); }
__device__ __forceinline__ f32x4 mfma(v4u a, v4u b, f32x4 c) {
  return __builtin_amdgcn_mfma_f32_16x16x32_bf16(__builtin_bit_cast(bf16x8, a), __builtin_bit_cast(bf16x8, b), c, 0, 0, 0);
}
__device__ __forceinline__ v4u amask(u32 dup, const v4u& m) { v4u r = {dup & m.x, dup & m.y, dup & m.z, dup & m.w}; return r; }

struct Tag { u32 f0, f1, f2, f3, keep; };
__device__ __forceinline__ Tag make_tag(u64 T) {
  const u32 tl = __builtin_amdgcn_readfirstlane((u32)T), th = __builtin_amdgcn_readfirstlane((u32)(T >> 32));
  const u64 Tu = ((u64)th << 32) | tl;
  Tag tg; tg.f0 = tl & TAGM; tg.f1 = ((u32)(Tu >> 10) + tl) & TAGM; tg.f2 = ((u32)(Tu >> 20) + tl) & TAGM; tg.f3 = ((u32)(Tu >> 30) + tl) & TAGM;
  tg.keep = ~TAGM;
  asm volatile("" : "+s"(tg.f0), "+s"(tg.f1), "+s"(tg.f2), "+s"(tg.f3), "+v"(tg.keep));
  return tg;
}
__device__ __forceinline__ v4u tag_pack(f32x4 v, const Tag& t) {
  v4u r = {(asu(v.x) & t.keep) | t.f0, (asu(v.y) & t.keep) | t.f1, (asu(v.z) & t.keep) | t.f2, (asu(v.w) & t.keep) | t.f3};
  return r;
}
__device__ __forceinline__ u32 tag_acc(u32 acc, v4u g, const Tag& t) {
  acc = __builtin_amdgcn_bitop3_b32(g.x, t.f0, acc, 0xBE); acc = __builtin_amdgcn_bitop3_b32(g.y, t.f1, acc, 0xBE);
  acc = __builtin_amdgcn_bitop3_b32(g.z, t.f2, acc, 0xBE); acc = __builtin_amdgcn_bitop3_b32(g.w, t.f3, acc, 0xBE);
  return acc;
}
__device__ __forceinline__ bool any_bad(u32 acc) { return __builtin_amdgcn_ballot_w64((acc & TAGM) != 0) != 0; }
__device__ __forceinline__ float untag(u32 w) { return asf(w & ~TAGM); }
__device__ __forceinline__ f32x4 untag4(v4u g) { f32x4 r = {untag(g.x), untag(g.y), untag(g.z), untag(g.w)}; return r; }

#define RSRC(ptr, bytes) __builtin_amdgcn_make_buffer_rsrc((void*)(ptr), 0, (int)(bytes), 0x00020000)
__device__ __forceinline__ v4u ld_stream(__amdgpu_buffer_rsrc_t r, u32 voff, u32 soff) { return __builtin_amdgcn_raw_buffer_load_b128(r, voff, soff, AUX_NT); }
__device__ __forceinline__ v4u ld_dev(__amdgpu_buffer_rsrc_t r, u32 voff, u32 soff) { return __builtin_amdgcn_raw_buffer_load_b128(r, voff, soff, AUX_SC1); }
__device__ __forceinline__ void st_dev(v4u v, __amdgpu_buffer_rsrc_t r, u32 voff, u32 soff) { __builtin_amdgcn_raw_buffer_store_b128(v, r, voff, soff, AUX_SC1); }
__device__ __forceinline__ u32 lds_ld(LAS u32* p) { return __hip_atomic_load(p, __ATOMIC_RELAXED, __HIP_MEMORY_SCOPE_WORKGROUP); }
__device__ __forceinline__ void lds_inc(LAS u32* p) { asm volatile("ds_add_u32 %0, %1" ::"v"((u32)(size_t)p), "v"(1u) : "memory"); }

__device__ __forceinline__ void ln_slice(v4u xa, v4u xb, u32 xo, int lane, LAS u32* xs) {
  float xv[16];
#pragma unroll
  for (int k = 0; k < 4; ++k) { xv[2 * k] = lo_f(xa[k]); xv[2 * k + 1] = hi_f(xa[k]); xv[8 + 2 * k] = lo_f(xb[k]); xv[9 + 2 * k] = hi_f(xb[k]); }
  float s0 = 0.f, s1 = 0.f;
#pragma unroll
  for (int k = 0; k < 8; ++k) { s0 += xv[k]; s1 += xv[8 + k]; }
  const float mean = xrow_sum(row_sum16(s0 + s1)) * (1.f / D_);
  float v1 = 0.f, v2 = 0.f;
#pragma unroll
  for (int k = 0; k < 8; ++k) { const float d1 = xv[k] - mean, d2 = xv[8 + k] - mean; v1 = fmaf(d1, d1, v1); v2 = fmaf(d2, d2, v2); }
  const float var = xrow_sum(row_sum16(lane < 16 ? v1 + v2 : v1)) * (1.f / D_);
  const float mb = rbf(mean), inv = rcp(rbf(__builtin_amdgcn_sqrtf(rbf(rbf(var) + 1e-5f))));
  const float o0 = rbf(lo_f(xo) - mb) * inv, o1 = rbf(hi_f(xo) - mb) * inv;
  v2u o = {pk_bf16(o0, o0), pk_bf16(o1, o1)};
  if (lane < 20) *(LAS v2u*)(xs + 2 * lane) = o;
  asm volatile("s_waitcnt lgkmcnt(0)" ::: "memory");
}

__global__ void __launch_bounds__(384, 2) fused_decode(Params p) {
  __shared__ __attribute__((aligned(16))) u32 lds[80 + 64 + 4 + 4 * 32 * 68];
  LAS u32* xs = (LAS u32*)lds;
  LAS u32* qb = xs + 80;
  LAS u32* cnt = qb + 64;

  const int tid = threadIdx.x, lane = tid & 63;
  const int wave = __builtin_amdgcn_readfirstlane(tid >> 6);
  const int b = blockIdx.x, h = b >> 3, j = b & 7;
  const int c = lane & 15, g = lane >> 4;
  const bool ffn = h < 27;
  v4u msk;
  {
    const u32 half = (c & 1) ? 0xffff0000u : 0x0000ffffu; const int r = (c & 7) >> 1;
    msk.x = r == 0 ? half : 0u; msk.y = r == 1 ? half : 0u; msk.z = r == 2 ? half : 0u; msk.w = r == 3 ? half : 0u;
  }
  u32 l16 = lane * 16, xoo = (lane < 20 ? lane : 0) * 4, wqo = g * (10 * 8192) + c * 16;
  asm volatile("" : "+v"(msk.x), "+v"(msk.y), "+v"(msk.z), "+v"(msk.w), "+v"(l16), "+v"(xoo), "+v"(wqo));
  SB();
  asm volatile("" : "+s"(p.x), "+s"(p.kc), "+s"(p.vc), "+s"(p.wq), "+s"(p.wo), "+s"(p.w1), "+s"(p.w2), "+s"(p.out), "+s"(p.ws));
  const __amdgpu_buffer_rsrc_t rws = RSRC(p.ws, WS_END);

  if (__builtin_expect(wave < 2, 1)) {
    __builtin_amdgcn_s_setprio(3);
    const __amdgpu_buffer_rsrc_t rx = RSRC(p.x, D_ * 2);
    const __amdgpu_buffer_rsrc_t rwq = RSRC(p.wq, D_ * 4096 * 2);
    const int row0 = 80 * j + 40 * wave;
    const v4u xa = __builtin_amdgcn_raw_buffer_load_b128(rx, l16, 0, 0);
    const v4u xb = __builtin_amdgcn_raw_buffer_load_b128(rx, l16 + 1024, 0, 0);
    const u32 xo = __builtin_amdgcn_raw_buffer_load_b32(rx, xoo, row0 * 2, 0);
    const u64 epoch = __hip_atomic_load((GAS u64*)(p.ws + WS_EPOCH), __ATOMIC_RELAXED, __HIP_MEMORY_SCOPE_AGENT);
    SB();
    v4u aq[10];
#pragma unroll
    for (int s = 0; s < 10; ++s) aq[s] = ld_stream(rwq, wqo, (row0 + s) * 8192 + h * 256);
    SB();
    const v2u xr = __builtin_amdgcn_raw_buffer_load_b64(rx, 0, (b < 160 ? b : 0) * 8, 0);
    if (lane < 4) cnt[lane] = 0;
    BAR();
    ln_slice(xa, xb, xo, lane, xs + 40 * wave);
    u32 xd[10];
#pragma unroll
    for (int s2 = 0; s2 < 5; ++s2) { const v2u t = *(LAS v2u*)(xs + 40 * wave + 10 * g + 2 * s2); xd[2 * s2] = t.x; xd[2 * s2 + 1] = t.y; }
    const Tag tg = make_tag(epoch + 1);
    u32 gvo = (2 * c + g) * 16, qso = WS_Q + (h * 16 + 2 * j + wave) * WQ_P;
    asm volatile("" : "+v"(gvo), "+s"(qso));
    f32x4 dq = {0.f, 0.f, 0.f, 0.f};
#pragma unroll
    for (int s = 0; s < 10; ++s) dq = mfma(amask(xd[s], msk), aq[s], dq);
    if (lane < 32) st_dev(tag_pack(dq, tg), rws, gvo, qso);
    __builtin_amdgcn_s_sleep(QDELAY);
    {
      const u32 qo = g * WQ_P + (16 * wave + c) * 16, qbase = WS_Q + h * 16 * WQ_P;
      v4u gq[4]; int spin = 0;
      do {
        MEMFENCE();
#pragma unroll
        for (int k = 0; k < 4; ++k) gq[k] = ld_dev(rws, qo, qbase + k * 4 * WQ_P);
      } while (any_bad(tag_acc(tag_acc(tag_acc(tag_acc(0u, gq[0], tg), gq[1], tg), gq[2], tg), gq[3], tg)) && ++spin < SPIN_CAP);
      const float r = xrow_sum4((untag4(gq[0]) + untag4(gq[1])) + (untag4(gq[2]) + untag4(gq[3])));
      ((LAS u16*)qb)[4 * (16 * wave + c) + g] = (u16)pk_bf16(r, r);
      MEMFENCE();
      if (lane == 0) lds_inc(cnt);
    }
    const __amdgpu_buffer_rsrc_t rw1 = RSRC(p.w1, ffn ? D_ * FF * 2 : 0);
    v4u ah[5];
#pragma unroll
    for (int s = 0; s < 5; ++s) ah[s] = ld_stream(rw1, (10 * g + 5 * (c >> 3)) * (FF * 2) + (c & 7) * 16, (row0 + s) * (FF * 2) + h * 128);
    SB();
    { int spin = 0; while (lds_ld(cnt + 2) < 4 && ++spin < SPIN_CAP) __builtin_amdgcn_s_sleep(1); }
    const __amdgpu_buffer_rsrc_t rwo = RSRC(p.wo, 4096 * D_ * 2);
    const __amdgpu_buffer_rsrc_t rw2 = RSRC(p.w2, ffn ? FF * D_ * 2 : 0);
    v4u bo[3][4], b2[3][2];
#define OLOADS(N0, N1) _Pragma("unroll") for (int n = N0; n < N1; ++n) { \
      const int k = n / 6, r = n % 6; \
      const bool valid = k < 2 || wave == 0; \
      const u32 so = valid ? (h * 128 + 16 * j) * 1280 + (3 * wave + k) * 256 : 0x70000000u, s2 = valid ? (h * 64 + 8 * j) * 1280 + (3 * wave + k) * 256 : 0x70000000u; \
      if (r < 2) b2[k][r] = ld_stream(rw2, (2 * g + r) * 1280 + c * 16, s2); \
      else bo[k][r - 2] = ld_stream(rwo, (4 * g + r - 2) * 1280 + c * 16, so); }
    OLOADS(0, OEARLY)
    SB();
    f32x4 dh = {0.f, 0.f, 0.f, 0.f};
#pragma unroll
    for (int s = 0; s < 5; ++s) dh = mfma(amask((c >> 3) ? xd[5 + s] : xd[s], msk), ah[s], dh);
#pragma unroll
    for (int k = 0; k < 4; ++k) {
      auto t = __builtin_amdgcn_permlane32_swap(asu(dh[k]), asu(dh[k]), false, false);
      dh[k] = asf(t[0]) + dpp<ROR8>(asf(t[1]));
    }
    if (c < 8 && g < 2) st_dev(tag_pack(dh, tg), rws, (2 * c + g) * 16, __builtin_amdgcn_readfirstlane(WS_H + (h * 16 + 2 * j + wave) * WH_P));
    { int spin = 0; while (lds_ld(cnt + 1) < 4 && ++spin < SPIN_CAP) __builtin_amdgcn_s_sleep(1); }
    SB();
    OLOADS(OEARLY, 18)
    SB();
    __builtin_amdgcn_s_sleep(ADELAY);
    const u32 aoff = c * WA_P + j * 80 + g * 16, moff = c * WA_P + j * 80 + 64, abase = WS_A + h * 32 * WA_P;
    const u32 hoff = c * WH_P + (2 * j + (g >> 1)) * 16, hbase = WS_H + h * 16 * WH_P;
    u32 yo = (2 * c + g) * 4096, yb = WS_Y + wave * 96 * 4096 + b * 16;
    asm volatile("" : "+v"(yo), "+s"(yb));
    v4u go0, go1, gm0, gm1, gh; int spin = 0;
    do {
      MEMFENCE();
      gh = ld_dev(rws, hoff, hbase); gm0 = ld_dev(rws, moff, abase); gm1 = ld_dev(rws, moff, abase + 16 * WA_P);
      go0 = ld_dev(rws, aoff, abase); go1 = ld_dev(rws, aoff, abase + 16 * WA_P);
    } while (any_bad(tag_acc(tag_acc(tag_acc(tag_acc(tag_acc(0u, gh, tg), gm0, tg), gm1, tg), go0, tg), go1, tg)) && ++spin < SPIN_CAP);
    {
      const bool b3 = c & 8, b2_ = c & 4;
      const float h0 = untag((g & 1) ? gh.z : gh.x), h1 = untag((g & 1) ? gh.w : gh.y);
      float hs = (b3 ? h1 : h0) + dpp<ROR8>(b3 ? h0 : h1);
      hs += dpp<HMIR>(hs); hs += dpp<XOR2>(hs); hs += dpp<XOR1>(hs);
      const float ht = rbf(hs), ha = ht * rcp(1.f + ex2(-ht * LOG2E));
      const u32 hd = pk_bf16(ha, ha);
      const float m0 = untag(gm0.x), l0 = untag(gm0.y), m1 = untag(gm1.x), l1 = untag(gm1.y);
      const float nM = -max_ror1(max_ror2(max_ror4(max_ror8(vmax(m0, m1))))) * LOG2E;
      const float f0 = ex2(fmaf(m0, LOG2E, nM)), f1 = ex2(fmaf(m1, LOG2E, nM));
      const float inv = rcp(row_sum16(fmaf(l0, f0, l1 * f1)));
      const f32x4 o = untag4(go0) * f0 + untag4(go1) * f1;
      const float u0 = (b3 ? o.z : o.x) + dpp<ROR8>(b3 ? o.x : o.z), u1 = (b3 ? o.w : o.y) + dpp<ROR8>(b3 ? o.y : o.w);
      float w = (b2_ ? u1 : u0) + dpp<HMIR>(b2_ ? u0 : u1);
      w += dpp<XOR2>(w); w += dpp<XOR1>(w);
      w *= inv;
      const u32 od = pk_bf16(w, w);
      const v4u am[6] = {amask(row_bcast<0>(hd), msk), amask(row_bcast<8>(hd), msk),
                         amask(row_bcast<0>(od), msk), amask(row_bcast<4>(od), msk), amask(row_bcast<8>(od), msk), amask(row_bcast<12>(od), msk)};
      f32x4 y[3];
#pragma unroll
      for (int k = 0; k < 3; ++k) y[k] = (f32x4){0.f, 0.f, 0.f, 0.f};
#pragma unroll
      for (int s = 0; s < 6; ++s)
#pragma unroll
        for (int k = 0; k < 3; ++k) y[k] = mfma(am[s], s < 2 ? b2[k][s] : bo[k][s - 2], y[k]);
      if (lane < 32) {
        st_dev(tag_pack(y[0], tg), rws, yo, yb);
        st_dev(tag_pack(y[1], tg), rws, yo, yb + 32 * 4096);
        if (wave == 0) st_dev(tag_pack(y[2], tg), rws, yo, yb + 64 * 4096);
      }
    }
    if (wave == 0 && b < 160) {
      __builtin_amdgcn_s_sleep(FDELAY);
      v4u gy[4];
#pragma unroll
      for (int k = 0; k < 4; ++k) gy[k] = ld_dev(rws, l16 + 1024 * k, WS_Y + b * 4096);
      for (int spin2 = 0;;) {
        u32 bad[4];
#pragma unroll
        for (int k = 0; k < 4; ++k) bad[k] = tag_acc(0u, gy[k], tg) & TAGM;
        if (__builtin_amdgcn_ballot_w64((bad[0] | bad[1] | bad[2] | bad[3]) != 0) == 0 || ++spin2 >= SPIN_CAP) break;
        MEMFENCE();
#pragma unroll
        for (int k = 0; k < 4; ++k) if (bad[k]) gy[k] = ld_dev(rws, l16 + 1024 * k, WS_Y + b * 4096);
      }
      const float r = row_sum16(xrow_sum4((untag4(gy[0]) + untag4(gy[1])) + (untag4(gy[2]) + untag4(gy[3]))));
      const u32 xw = g < 2 ? xr.x : xr.y;
      const float res = r + ((g & 1) ? hi_f(xw) : lo_f(xw));
      if (c == 0) __hip_atomic_store((GAS u16*)p.out + 4 * b + g, (u16)pk_bf16(res, res), __ATOMIC_RELAXED, __HIP_MEMORY_SCOPE_AGENT);
      if (b == 0 && lane == 0) __hip_atomic_store((GAS u64*)(p.ws + WS_EPOCH), epoch + 1, __ATOMIC_RELAXED, __HIP_MEMORY_SCOPE_AGENT);
    }
  } else {
    const int kw = wave - 2;
    const __amdgpu_buffer_rsrc_t rk = RSRC(p.kc, NH * SK * DH * 2);
    const __amdgpu_buffer_rsrc_t rv = RSRC(p.vc, NH * SK * DH * 2);
    const u32 kvs = (u32)((h * SK + (SK - WIN) + 128 * j + 32 * ((kw + h + j) & 3)) * 256);
    const u32 kvo = g * 256 + c * 16, rot = (2 * h + (j >> 1) + 2 * j) & 7;
    const u64 epoch = __hip_atomic_load((GAS u64*)(p.ws + WS_EPOCH), __ATOMIC_RELAXED, __HIP_MEMORY_SCOPE_AGENT);
    BAR();
    LAS u32* kt = cnt + 4 + kw * (32 * 68);
    LAS u32* kwr = kt + g * (4 * 68) + c * 4;
    v4u kv[16];
#pragma unroll
    for (int n = 0; n < 16; ++n) {
      if (n >= KVWIN) {
        if (n - KVWIN < 8) *(LAS v4u*)(kwr + (16 * ((n - KVWIN) >> 2) + ((n - KVWIN) & 3)) * 68) = kv[n - KVWIN];
        else asm volatile("" ::"v"(kv[n - KVWIN]));
        SB();
      }
      kv[n] = ld_stream(n < 8 ? rk : rv, kvo, kvs + ((n + rot) & 7) * 1024);
      SB();
    }
    if (lane == 0) lds_inc(cnt + 2);
#pragma unroll
    for (int n = 16 - KVWIN; n < 8; ++n) *(LAS v4u*)(kwr + (16 * (n >> 2) + (n & 3)) * 68) = kv[n];
    v4u kf[2][4];
#pragma unroll
    for (int t = 0; t < 2; ++t)
#pragma unroll
      for (int s4 = 0; s4 < 4; ++s4) kf[t][s4] = *(LAS v4u*)(kt + (16 * t + c) * 68 + s4 * 16 + g * 4);
    asm volatile("" : "+v"(kf[0][0]), "+v"(kf[0][1]), "+v"(kf[0][2]), "+v"(kf[0][3]), "+v"(kf[1][0]), "+v"(kf[1][1]), "+v"(kf[1][2]), "+v"(kf[1][3]));
    const Tag tg = make_tag(epoch + 1);
    const u32 gi = 2 * c + g;
    u32 pvo = lane < 32 ? (gi >> 2) * 80 + (gi & 3) * 16 : (lane - 32) * 80 + 64, pso = WS_A + (h * 32 + 4 * j + kw) * WA_P;
    asm volatile("" : "+v"(pvo), "+s"(pso));
    { int spin = 0; while (lds_ld(cnt) < 2 && ++spin < SPIN_CAP) __builtin_amdgcn_s_sleep(1); }
    MEMFENCE();
    {
      f32x4 d0 = {0.f, 0.f, 0.f, 0.f}, d1 = {0.f, 0.f, 0.f, 0.f};
      v4u qf[4];
#pragma unroll
      for (int s4 = 0; s4 < 4; ++s4) qf[s4] = *(LAS v4u*)(qb + 16 * s4 + 4 * g);
      SB();
#pragma unroll
      for (int s4 = 0; s4 < 4; ++s4) { d0 = mfma(kf[0][s4], qf[s4], d0); d1 = mfma(kf[1][s4], qf[s4], d1); }
      const bool b0 = c & 1, b1 = c & 2, b2 = c & 4;
      const float x0 = b0 ? d0.y : d0.x, x1 = b0 ? d0.w : d0.z, x2 = b0 ? d1.y : d1.x, x3 = b0 ? d1.w : d1.z;
      const float y0 = b1 ? x1 : x0, y1 = b1 ? x3 : x2;
      const float d = b2 ? y1 : y0;
      const float s = rbf(rbf(d) * 0.08838834764831845f);
      const float m = xrow_max(max_hmir(max_xor2(max_xor1(s))));
      const float pe = ex2((s - m) * LOG2E);
      const u32 pd = pk_bf16(pe, pe);
      const v4u am[8] = {amask(row_bcast<0>(pd), msk), amask(row_bcast<1>(pd), msk), amask(row_bcast<2>(pd), msk), amask(row_bcast<3>(pd), msk),
                         amask(row_bcast<4>(pd), msk), amask(row_bcast<5>(pd), msk), amask(row_bcast<6>(pd), msk), amask(row_bcast<7>(pd), msk)};
      float l = pe + dpp<XOR1>(pe); l += dpp<XOR2>(l); l += dpp<HMIR>(l);
      l = xrow_sum(l);
      SB();
      f32x4 o = {0.f, 0.f, 0.f, 0.f};
#pragma unroll
      for (int n = 0; n < 8; ++n) o = mfma(am[n], kv[8 + n], o);
      if (lane >= 32) { o.x = m; o.y = l; o.z = m; o.w = l; }
      if (lane < 40) st_dev(tag_pack(o, tg), rws, pvo, pso);
      MEMFENCE();
      if (lane == 0) lds_inc(cnt + 1);
    }
  }
}

extern "C" void kernel_launch(void* const* d_in, const int* in_sizes, int n_in, void* d_out, int out_size, void* d_ws, size_t ws_size, hipStream_t stream) {
  Params p;
  p.x = (const u16*)d_in[0]; p.kc = (const u16*)d_in[1]; p.vc = (const u16*)d_in[2]; p.wq = (const u16*)d_in[3]; p.wo = (const u16*)d_in[4];
  p.w1 = (const u16*)d_in[6]; p.w2 = (const u16*)d_in[7]; p.out = (u16*)d_out; p.ws = (unsigned char*)d_ws;
  fused_decode<<<256, 384, 0, stream>>>(p);
}
```
